```python
import torch

def _softmax(x, axis=-1):
    return torch.softmax(x, dim=axis)
def _gelu(x):
    return torch.nn.functional.gelu(x, approximate="tanh")
def _silu(x):
    return torch.nn.functional.silu(x)
def _sigmoid(x):
    return torch.sigmoid(x)
def _layernorm(x, axis=-1, eps=1e-5):
    m = x.mean(dim=axis, keepdim=True)
    v = x.var(dim=axis, keepdim=True, unbiased=False)
    return (x - m) / torch.sqrt(v + eps)
def _scatter_add(tgt, idx, src, axis=0):
    return tgt.clone().index_add_(axis, idx, src)
def _segment_sum(src, seg, n):
    out = torch.zeros((n,) + src.shape[1:], dtype=src.dtype)
    return out.index_add_(0, seg, src)
def _pad(x, pw):
    flat = tuple(v for pair in reversed(pw) for v in pair)
    return torch.nn.functional.pad(x, flat)

kernel_name = 'interleave_layers__sliding_block_cached__sliding_block_cached__decode'

def setup_inputs():

    _w = (lambda a: (lambda t: t.to(torch.bfloat16) if t.is_floating_point() else t)(torch.as_tensor(a)))
    def _A_setup():
        B, Sq, D = 4, 1, 512
        Hq, Hkv, Sk, Dh = 32, 8, 4096, 128
        assert Hq % Hkv == 0, (Hq, Hkv)
        return {
            "x": _w((lambda *s: torch.randn(*s, dtype=torch.bfloat16) * 0.05)(B, Sq, D)),
            "k_cache": _w((lambda *s: torch.randn(*s, dtype=torch.bfloat16) * 0.8)(B, Hkv, Sk, Dh)),
            "v_cache": _w((lambda *s: torch.randn(*s, dtype=torch.bfloat16) * 3.2)(B, Hkv, Sk, Dh)),
            "wq": _w((lambda *s: torch.randn(*s, dtype=torch.bfloat16) * 0.8)(D, Hq * Dh) * (D ** -0.5)),
            "wo": _w((lambda *s: torch.randn(*s, dtype=torch.bfloat16) * 3.2)(Hq * Dh, D) * ((Hq * Dh) ** -0.5)),
        }
    _A_in = _A_setup()

    def _B_setup():
        B, Sq, D = 4, 1, 512
        Hq, Hkv, Sk, Dh = 32, 8, 4096, 128
        assert Hq % Hkv == 0, (Hq, Hkv)
        return {
            "x": _w((lambda *s: torch.randn(*s, dtype=torch.bfloat16) * 0.05)(B, Sq, D)),
            "k_cache": _w((lambda *s: torch.randn(*s, dtype=torch.bfloat16) * 1.6)(B, Hkv, Sk, Dh)),
            "v_cache": _w((lambda *s: torch.randn(*s, dtype=torch.bfloat16) * 1.6)(B, Hkv, Sk, Dh)),
            "wq": _w((lambda *s: torch.randn(*s, dtype=torch.bfloat16) * 1.6)(D, Hq * Dh) * (D ** -0.5)),
            "wo": _w((lambda *s: torch.randn(*s, dtype=torch.bfloat16) * 1.6)(Hq * Dh, D) * ((Hq * Dh) ** -0.5)),
        }
    _B_in = _B_setup()

    return {
        "A_x": _A_in["x"],
        "A_k_cache": _A_in["k_cache"],
        "A_v_cache": _A_in["v_cache"],
        "A_wq": _A_in["wq"],
        "A_wo": _A_in["wo"],
        "B_x": _B_in["x"],
        "B_k_cache": _B_in["k_cache"],
        "B_v_cache": _B_in["v_cache"],
        "B_wq": _B_in["wq"],
        "B_wo": _B_in["wo"],
    }

def reference(A_x, A_k_cache, A_v_cache, A_wq, A_wo, B_x, B_k_cache, B_v_cache, B_wq, B_wo):
    def _A_ref(x, k_cache, v_cache, wq, wo):
        B, Sq, D = x.shape
        Hq, Dh = 32, 128
        Hkv, Sk = k_cache.shape[1], k_cache.shape[2]
        W = min(4096, max(256, Sk // 4))
        rep = Hq // Hkv
        q = (lambda x, *d: x.permute(*d))((x @ wq).reshape(B, Sq, Hq, Dh), 0, 2, 1, 3)
        k = (lambda x, n, axis: torch.repeat_interleave(x, n, dim=axis))(k_cache[:, :, Sk - W:, :], rep, axis=1)
        v = (lambda x, n, axis: torch.repeat_interleave(x, n, dim=axis))(v_cache[:, :, Sk - W:, :], rep, axis=1)
        scores = torch.einsum("bhqd,bhkd->bhqk", q, k) * (Dh ** -0.5)
        p = _softmax(scores, axis=-1)
        o = torch.einsum("bhqk,bhkd->bhqd", p, v)
        return (lambda x, *d: x.permute(*d))(o, 0, 2, 1, 3).reshape(B, Sq, Hq * Dh) @ wo

    def _B_ref(x, k_cache, v_cache, wq, wo):
        B, Sq, D = x.shape
        Hq, Dh = 32, 128
        Hkv, Sk = k_cache.shape[1], k_cache.shape[2]
        W = min(4096, max(256, Sk // 4))
        rep = Hq // Hkv
        q = (lambda x, *d: x.permute(*d))((x @ wq).reshape(B, Sq, Hq, Dh), 0, 2, 1, 3)
        k = (lambda x, n, axis: torch.repeat_interleave(x, n, dim=axis))(k_cache[:, :, Sk - W:, :], rep, axis=1)
        v = (lambda x, n, axis: torch.repeat_interleave(x, n, dim=axis))(v_cache[:, :, Sk - W:, :], rep, axis=1)
        scores = torch.einsum("bhqd,bhkd->bhqk", q, k) * (Dh ** -0.5)
        p = _softmax(scores, axis=-1)
        o = torch.einsum("bhqk,bhkd->bhqd", p, v)
        return (lambda x, *d: x.permute(*d))(o, 0, 2, 1, 3).reshape(B, Sq, Hq * Dh) @ wo

    x = A_x
    x = x + _A_ref(_layernorm(x, axis=-1), A_k_cache, A_v_cache, A_wq, A_wo)
    x = x + _B_ref(_layernorm(x, axis=-1), B_k_cache, B_v_cache, B_wq, B_wo)
    x = x + _A_ref(_layernorm(x, axis=-1), A_k_cache, A_v_cache, A_wq, A_wo)
    x = x + _B_ref(_layernorm(x, axis=-1), B_k_cache, B_v_cache, B_wq, B_wo)
    return x
```

```cpp
#include <hip/hip_runtime.h>
#include <hip/hip_bf16.h>
#include <stdint.h>

typedef unsigned short u16;
__device__ __forceinline__ float bf2f(u16 b) { return __builtin_bit_cast(float, (unsigned)b << 16); }
__device__ __forceinline__ float rbf(float f) {
    unsigned u = __builtin_bit_cast(unsigned, f);
    u += 0x7FFFu + ((u >> 16) & 1u);
    return __builtin_bit_cast(float, u & 0xFFFF0000u);
}
constexpr int B = 4, D = 512, HQ = 32, HKV = 8, SK = 4096, DH = 128, W = 1024, NS = 8;

__global__ void __launch_bounds__(256) k_q(const u16* x0, const float* xprev, const float* part, float* xnew, const u16* wq, float* q) {
    __shared__ float xs[4][512];
    __shared__ float red[4][4][64];
    __shared__ float st[4][2];
    int tid = threadIdx.x;
    for (int i = tid; i < 2048; i += 256) {
        float v;
        if (part) {
            int b = i >> 9, c = i & 511;
            float s = 0.f;
            for (int g = 0; g < 8; ++g) s += part[(g * 4 + b) * 512 + c];
            v = rbf(xprev[i] + rbf(s));
        } else v = bf2f(x0[i]);
        xs[i >> 9][i & 511] = v;
        if (blockIdx.x == 0) xnew[i] = v;
    }
    __syncthreads();
    {
        int b = tid >> 6, l = tid & 63;
        float s = 0.f;
        for (int c = l; c < 512; c += 64) s += xs[b][c];
        for (int o = 32; o; o >>= 1) s += __shfl_xor(s, o);
        float mean = s * (1.f / 512.f);
        float v = 0.f;
        for (int c = l; c < 512; c += 64) { float d = xs[b][c] - mean; v += d * d; }
        for (int o = 32; o; o >>= 1) v += __shfl_xor(v, o);
        v *= (1.f / 512.f);
        float mb = rbf(mean), vb = rbf(v);
        float den = rbf(sqrtf(rbf(vb + 1e-5f)));
        for (int c = l; c < 512; c += 64) xs[b][c] = rbf(rbf(xs[b][c] - mb) / den);
    }
    __syncthreads();
    int col = blockIdx.x * 64 + (tid & 63), kq = tid >> 6;
    float acc[4] = {0, 0, 0, 0};
    for (int c = kq * 128; c < kq * 128 + 128; ++c) {
        float w = bf2f(wq[(size_t)c * 4096 + col]);
        for (int b = 0; b < 4; ++b) acc[b] += xs[b][c] * w;
    }
    for (int b = 0; b < 4; ++b) red[kq][b][tid & 63] = acc[b];
    __syncthreads();
    { int b = tid >> 6, l = tid & 63;
      float s = red[0][b][l] + red[1][b][l] + red[2][b][l] + red[3][b][l];
      q[b * 4096 + blockIdx.x * 64 + l] = rbf(s); }
}
__global__ void __launch_bounds__(256) k_attn(const float* q, const u16* kc, const u16* vc, float* po, float* pml) {
    __shared__ float qs[4][128];
    __shared__ float sc[4][128];
    __shared__ float ml[4][2];
    int bid = blockIdx.x, s = bid & 7, kvh = (bid >> 3) & 7, b = bid >> 6;
    int tid = threadIdx.x;
    for (int i = tid; i < 512; i += 256) qs[i >> 7][i & 127] = q[b * 4096 + kvh * 512 + i];
    __syncthreads();
    const u16* kb = kc + ((size_t)(b * 8 + kvh) * SK + (SK - W) + s * 128) * 128;
    const u16* vb = vc + ((size_t)(b * 8 + kvh) * SK + (SK - W) + s * 128) * 128;
    for (int i = tid; i < 512; i += 256) {
        int h = i >> 7, key = i & 127;
        float a = 0.f;
        for (int d = 0; d < 128; ++d) a += qs[h][d] * bf2f(kb[key * 128 + d]);
        sc[h][key] = rbf(rbf(a) * rbf(0.08838834764831845f));
    }
    __syncthreads();
    { int h = tid >> 6, l = tid & 63;
      float m = fmaxf(sc[h][l], sc[h][l + 64]);
      for (int o = 32; o; o >>= 1) m = fmaxf(m, __shfl_xor(m, o));
      float e0 = __expf(sc[h][l] - m), e1 = __expf(sc[h][l + 64] - m);
      float su = e0 + e1;
      for (int o = 32; o; o >>= 1) su += __shfl_xor(su, o);
      sc[h][l] = e0; sc[h][l + 64] = e1;
      if (l == 0) { pml[(bid * 4 + h) * 2] = m; pml[(bid * 4 + h) * 2 + 1] = su; } }
    __syncthreads();
    for (int i = tid; i < 512; i += 256) {
        int h = i >> 7, d = i & 127;
        float a = 0.f;
        for (int key = 0; key < 128; ++key) a += sc[h][key] * bf2f(vb[key * 128 + d]);
        po[(size_t)bid * 512 + i] = a;
    }
}
__global__ void __launch_bounds__(256) k_o(const float* po, const float* pml, const u16* wo, float* part) {
    __shared__ float os[4][512];
    __shared__ float red[4][4][64];
    int kvh = blockIdx.x >> 3, cg = blockIdx.x & 7, tid = threadIdx.x;
    for (int i = tid; i < 2048; i += 256) {
        int b = i >> 9, r = i & 511, h = r >> 7;
        float M = -INFINITY;
        for (int s = 0; s < 8; ++s) M = fmaxf(M, pml[(((b * 8 + kvh) * 8 + s) * 4 + h) * 2]);
        float L = 0.f, o = 0.f;
        for (int s = 0; s < 8; ++s) {
            int bid = (b * 8 + kvh) * 8 + s;
            float w = __expf(pml[(bid * 4 + h) * 2] - M);
            L += w * pml[(bid * 4 + h) * 2 + 1];
            o += w * po[(size_t)bid * 512 + r];
        }
        os[b][r] = rbf(o / L);
    }
    __syncthreads();
    int col = cg * 64 + (tid & 63), kq = tid >> 6;
    float acc[4] = {0, 0, 0, 0};
    for (int r = kq * 128; r < kq * 128 + 128; ++r) {
        float w = bf2f(wo[(size_t)(kvh * 512 + r) * 512 + col]);
        for (int b = 0; b < 4; ++b) acc[b] += os[b][r] * w;
    }
    for (int b = 0; b < 4; ++b) red[kq][b][tid & 63] = acc[b];
    __syncthreads();
    { int b = tid >> 6, l = tid & 63;
      part[(kvh * 4 + b) * 512 + cg * 64 + l] = red[0][b][l] + red[1][b][l] + red[2][b][l] + red[3][b][l]; }
}
__global__ void k_fin(const float* xprev, const float* part, u16* out) {
    int i = blockIdx.x * 256 + threadIdx.x;
    int b = i >> 9, c = i & 511;
    float s = 0.f;
    for (int g = 0; g < 8; ++g) s += part[(g * 4 + b) * 512 + c];
    float v = rbf(xprev[i] + rbf(s));
    out[i] = (u16)(__builtin_bit_cast(unsigned, v) >> 16);
}

extern "C" void kernel_launch(void* const* d_in, const int* in_sizes, int n_in, void* d_out, int out_size, void* d_ws, size_t ws_size, hipStream_t stream) {
    float* ws = (float*)d_ws;
    float* xb[2] = {ws, ws + 2048};
    float* q = ws + 4096;
    float* po = q + 16384;
    float* pml = po + 256 * 512;
    float* part = pml + 256 * 8;
    for (int l = 0; l < 4; ++l) {
        int o = (l & 1) * 5;
        const u16* kc = (const u16*)d_in[o + 1]; const u16* vc = (const u16*)d_in[o + 2];
        const u16* wq = (const u16*)d_in[o + 3]; const u16* wo = (const u16*)d_in[o + 4];
        k_q<<<64, 256, 0, stream>>>((const u16*)d_in[0], xb[(l + 1) & 1], l ? part : nullptr, xb[l & 1], wq, q);
        k_attn<<<256, 256, 0, stream>>>(q, kc, vc, po, pml);
        k_o<<<64, 256, 0, stream>>>(po, pml, wo, part);
    }
    k_fin<<<8, 256, 0, stream>>>(xb[1], part, (u16*)d_out);
}
```

```cpp
#include <hip/hip_runtime.h>
#include <hip/hip_bf16.h>
#include <stdint.h>

typedef unsigned short u16;
__device__ __forceinline__ float bf2f(u16 b) { return __builtin_bit_cast(float, (unsigned)b << 16); }
__device__ __forceinline__ float rbf(float f) {
    return f;
}
__device__ __forceinline__ float rbf2(float f) {
    unsigned u = __builtin_bit_cast(unsigned, f);
    u += 0x7FFFu + ((u >> 16) & 1u);
    return __builtin_bit_cast(float, u & 0xFFFF0000u);
}
constexpr int B = 4, D = 512, HQ = 32, HKV = 8, SK = 4096, DH = 128, W = 1024, NS = 8;

__global__ void __launch_bounds__(256) k_q(const u16* x0, const float* xprev, const float* part, float* xnew, const u16* wq, float* q) {
    __shared__ float xs[4][512];
    __shared__ float red[4][4][64];
    __shared__ float st[4][2];
    int tid = threadIdx.x;
    for (int i = tid; i < 2048; i += 256) {
        float v;
        if (part) {
            int b = i >> 9, c = i & 511;
            float s = 0.f;
            for (int g = 0; g < 8; ++g) s += part[(g * 4 + b) * 512 + c];
            v = rbf(xprev[i] + rbf(s));
        } else v = bf2f(x0[i]);
        xs[i >> 9][i & 511] = v;
        if (blockIdx.x == 0) xnew[i] = v;
    }
    __syncthreads();
    {
        int b = tid >> 6, l = tid & 63;
        float s = 0.f;
        for (int c = l; c < 512; c += 64) s += xs[b][c];
        for (int o = 32; o; o >>= 1) s += __shfl_xor(s, o);
        float mean = s * (1.f / 512.f);
        float v = 0.f;
        for (int c = l; c < 512; c += 64) { float d = xs[b][c] - mean; v += d * d; }
        for (int o = 32; o; o >>= 1) v += __shfl_xor(v, o);
        v *= (1.f / 512.f);
        float mb = rbf(mean), vb = rbf(v);
        float den = rbf(sqrtf(rbf(vb + 1e-5f)));
        for (int c = l; c < 512; c += 64) xs[b][c] = rbf(rbf(xs[b][c] - mb) / den);
    }
    __syncthreads();
    int col = blockIdx.x * 64 + (tid & 63), kq = tid >> 6;
    float acc[4] = {0, 0, 0, 0};
    for (int c = kq * 128; c < kq * 128 + 128; ++c) {
        float w = bf2f(wq[(size_t)c * 4096 + col]);
        for (int b = 0; b < 4; ++b) acc[b] += xs[b][c] * w;
    }
    for (int b = 0; b < 4; ++b) red[kq][b][tid & 63] = acc[b];
    __syncthreads();
    { int b = tid >> 6, l = tid & 63;
      float s = red[0][b][l] + red[1][b][l] + red[2][b][l] + red[3][b][l];
      q[b * 4096 + blockIdx.x * 64 + l] = rbf(s); }
}
__global__ void __launch_bounds__(256) k_attn(const float* q, const u16* kc, const u16* vc, float* po, float* pml) {
    __shared__ float qs[4][128];
    __shared__ float sc[4][128];
    __shared__ float ml[4][2];
    int bid = blockIdx.x, s = bid & 7, kvh = (bid >> 3) & 7, b = bid >> 6;
    int tid = threadIdx.x;
    for (int i = tid; i < 512; i += 256) qs[i >> 7][i & 127] = q[b * 4096 + kvh * 512 + i];
    __syncthreads();
    const u16* kb = kc + ((size_t)(b * 8 + kvh) * SK + (SK - W) + s * 128) * 128;
    const u16* vb = vc + ((size_t)(b * 8 + kvh) * SK + (SK - W) + s * 128) * 128;
    for (int i = tid; i < 512; i += 256) {
        int h = i >> 7, key = i & 127;
        float a = 0.f;
        for (int d = 0; d < 128; ++d) a += qs[h][d] * bf2f(kb[key * 128 + d]);
        sc[h][key] = rbf(rbf(a) * 0.08838834764831845f);
    }
    __syncthreads();
    { int h = tid >> 6, l = tid & 63;
      float m = fmaxf(sc[h][l], sc[h][l + 64]);
      for (int o = 32; o; o >>= 1) m = fmaxf(m, __shfl_xor(m, o));
      float e0 = __expf(sc[h][l] - m), e1 = __expf(sc[h][l + 64] - m);
      float su = e0 + e1;
      for (int o = 32; o; o >>= 1) su += __shfl_xor(su, o);
      sc[h][l] = e0; sc[h][l + 64] = e1;
      if (l == 0) { pml[(bid * 4 + h) * 2] = m; pml[(bid * 4 + h) * 2 + 1] = su; } }
    __syncthreads();
    for (int i = tid; i < 512; i += 256) {
        int h = i >> 7, d = i & 127;
        float a = 0.f;
        for (int key = 0; key < 128; ++key) a += sc[h][key] * bf2f(vb[key * 128 + d]);
        po[(size_t)bid * 512 + i] = a;
    }
}
__global__ void __launch_bounds__(256) k_o(const float* po, const float* pml, const u16* wo, float* part) {
    __shared__ float os[4][512];
    __shared__ float red[4][4][64];
    int kvh = blockIdx.x >> 3, cg = blockIdx.x & 7, tid = threadIdx.x;
    for (int i = tid; i < 2048; i += 256) {
        int b = i >> 9, r = i & 511, h = r >> 7;
        float M = -INFINITY;
        for (int s = 0; s < 8; ++s) M = fmaxf(M, pml[(((b * 8 + kvh) * 8 + s) * 4 + h) * 2]);
        float L = 0.f, o = 0.f;
        for (int s = 0; s < 8; ++s) {
            int bid = (b * 8 + kvh) * 8 + s;
            float w = __expf(pml[(bid * 4 + h) * 2] - M);
            L += w * pml[(bid * 4 + h) * 2 + 1];
            o += w * po[(size_t)bid * 512 + r];
        }
        os[b][r] = rbf(o / L);
    }
    __syncthreads();
    int col = cg * 64 + (tid & 63), kq = tid >> 6;
    float acc[4] = {0, 0, 0, 0};
    for (int r = kq * 128; r < kq * 128 + 128; ++r) {
        float w = bf2f(wo[(size_t)(kvh * 512 + r) * 512 + col]);
        for (int b = 0; b < 4; ++b) acc[b] += os[b][r] * w;
    }
    for (int b = 0; b < 4; ++b) red[kq][b][tid & 63] = acc[b];
    __syncthreads();
    { int b = tid >> 6, l = tid & 63;
      part[(kvh * 4 + b) * 512 + cg * 64 + l] = red[0][b][l] + red[1][b][l] + red[2][b][l] + red[3][b][l]; }
}
__global__ void k_fin(const float* xprev, const float* part, u16* out) {
    int i = blockIdx.x * 256 + threadIdx.x;
    int b = i >> 9, c = i & 511;
    float s = 0.f;
    for (int g = 0; g < 8; ++g) s += part[(g * 4 + b) * 512 + c];
    float v = rbf2(xprev[i] + rbf(s));
    out[i] = (u16)(__builtin_bit_cast(unsigned, v) >> 16);
}

extern "C" void kernel_launch(void* const* d_in, const int* in_sizes, int n_in, void* d_out, int out_size, void* d_ws, size_t ws_size, hipStream_t stream) {
    float* ws = (float*)d_ws;
    float* xb[2] = {ws, ws + 2048};
    float* q = ws + 4096;
    float* po = q + 16384;
    float* pml = po + 256 * 512;
    float* part = pml + 256 * 8;
    for (int l = 0; l < 4; ++l) {
        int o = (l & 1) * 5;
        const u16* kc = (const u16*)d_in[o + 1]; const u16* vc = (const u16*)d_in[o + 2];
        const u16* wq = (const u16*)d_in[o + 3]; const u16* wo = (const u16*)d_in[o + 4];
        k_q<<<64, 256, 0, stream>>>((const u16*)d_in[0], xb[(l + 1) & 1], l ? part : nullptr, xb[l & 1], wq, q);
        k_attn<<<256, 256, 0, stream>>>(q, kc, vc, po, pml);
        k_o<<<64, 256, 0, stream>>>(po, pml, wo, part);
    }
    k_fin<<<8, 256, 0, stream>>>(xb[1], part, (u16*)d_out);
}
```

```cpp
#include <hip/hip_runtime.h>
#include <hip/hip_bf16.h>
#include <stdint.h>
#include <type_traits>

#define MK_STAMPS 0

#define GAS __attribute__((address_space(1)))
#define LAS __attribute__((address_space(3)))
typedef unsigned short u16;
typedef unsigned v4u __attribute__((ext_vector_type(4)));
typedef unsigned v2u __attribute__((ext_vector_type(2)));
typedef float f32x4 __attribute__((ext_vector_type(4)));
typedef float f32x2 __attribute__((ext_vector_type(2)));
typedef short s16x4 __attribute__((ext_vector_type(4)));
typedef __bf16 bf16x8 __attribute__((ext_vector_type(8)));
typedef __bf16 bf16x2 __attribute__((ext_vector_type(2)));

namespace mk {
constexpr int D = 512, HQ = 32, HKV = 8, SK = 4096, DH = 128, W = 1024;
constexpr unsigned EMPTY = 0xFFFFFFFFu;
constexpr int AUX_SC1 = 16;
constexpr unsigned SPIN_CAP = 1u << 17;

constexpr int WS_GEN = 0;
constexpr int WS_HELLO = 1024;
constexpr int WS_QB = 4096;
constexpr int QB_L = 32 * 1024;
constexpr int WS_OB = WS_QB + 4 * QB_L;
constexpr int OB_P = 4 * 384, OB_G = 8 * OB_P, OB_L = 32 * OB_G;
constexpr int WS_XB = WS_OB + 4 * OB_L;
constexpr int XB_R = 8 * 2048, XB_B = 8 * XB_R, XB_L = 4 * XB_B;
constexpr int WS_END = WS_XB + 4 * XB_L;
constexpr int WS_STAMP = (WS_END + 4095) & ~4095;

constexpr int L_STGW = 0;
constexpr int L_STGV = 7 * 16384;
constexpr int L_EX = L_STGV + 32768;
constexpr int L_XS = L_EX;
constexpr int L_ONES = L_XS + 1024;
constexpr int L_QS = L_ONES + 1024;
constexpr int L_OS = L_QS + 1024;
constexpr int L_ST = L_OS + 1024;
constexpr int L_ML = L_ST + 64;
constexpr int L_OW = L_ML + 128 + 64;
constexpr int OW_H = 132 * 4, OW_W = 4 * OW_H;
constexpr int L_END = L_OW + 4 * OW_W;
constexpr int LDS_BYTES = 160 * 1024;
static_assert(L_END <= LDS_BYTES, "lds");
static_assert((L_OW % 16) == 0 && (OW_H % 16) == 0, "align");

struct Params {
    const u16* x; const u16* kc0; const u16* vc0; const u16* wq0; const u16* wo0;
    const u16* kc1; const u16* vc1; const u16* wq1; const u16* wo1;
    u16* out; unsigned char* ws;
};

__device__ __forceinline__ f32x4 mfma(v4u a, v4u b, f32x4 c) {
    return __builtin_amdgcn_mfma_f32_16x16x32_bf16(__builtin_bit_cast(bf16x8, a), __builtin_bit_cast(bf16x8, b), c, 0, 0, 0);
}
__device__ __forceinline__ unsigned cvtpk(float lo, float hi) {
    bf16x2 r = {(__bf16)lo, (__bf16)hi};
    return __builtin_bit_cast(unsigned, r);
}
__device__ __forceinline__ float bflo(unsigned u) { return __builtin_bit_cast(float, u << 16); }
__device__ __forceinline__ float bfhi(unsigned u) { return __builtin_bit_cast(float, u & 0xFFFF0000u); }
__device__ __forceinline__ float rbf(float f) { return bflo(cvtpk(f, f)); }
template <int CTRL> __device__ __forceinline__ float dpp(float x) {
    return __builtin_bit_cast(float, __builtin_amdgcn_mov_dpp(__builtin_bit_cast(int, x), CTRL, 0xf, 0xf, true));
}
constexpr int XOR1 = 0xB1, XOR2 = 0x4E, HMIR = 0x141, MIR = 0x140, ROR4 = 0x124, ROR8 = 0x128, SHL1 = 0x101;
__device__ __forceinline__ float x16_max(float x) {
    auto s = __builtin_amdgcn_permlane16_swap(__float_as_uint(x), __float_as_uint(x), false, false);
    x = fmaxf(__uint_as_float(s[0]), __uint_as_float(s[1]));
    auto t = __builtin_amdgcn_permlane32_swap(__float_as_uint(x), __float_as_uint(x), false, false);
    return fmaxf(__uint_as_float(t[0]), __uint_as_float(t[1]));
}
__device__ __forceinline__ float x16_sum(float x) {
    auto s = __builtin_amdgcn_permlane16_swap(__float_as_uint(x), __float_as_uint(x), false, false);
    x = __uint_as_float(s[0]) + __uint_as_float(s[1]);
    auto t = __builtin_amdgcn_permlane32_swap(__float_as_uint(x), __float_as_uint(x), false, false);
    return __uint_as_float(t[0]) + __uint_as_float(t[1]);
}
__device__ __forceinline__ float row16_sum(float x) {
    x += dpp<XOR1>(x); x += dpp<XOR2>(x); x += dpp<HMIR>(x); x += dpp<MIR>(x); return x;
}
__device__ __forceinline__ float swap32_add(float a, float b) {
    auto s = __builtin_amdgcn_permlane32_swap(__float_as_uint(a), __float_as_uint(b), false, false);
    return __uint_as_float(s[0]) + __uint_as_float(s[1]);
}
__device__ __forceinline__ float swap16_add(float a, float b) {
    auto s = __builtin_amdgcn_permlane16_swap(__float_as_uint(a), __float_as_uint(b), false, false);
    return __uint_as_float(s[0]) + __uint_as_float(s[1]);
}
__device__ __forceinline__ unsigned mx4(v4u v) { return max(max(v.x, v.y), max(v.z, v.w)); }
__device__ __forceinline__ unsigned noempty(unsigned u) { return min(u, 0xFFFFFFFEu); }

#define BAR() do { asm volatile("s_waitcnt lgkmcnt(0)" ::: "memory"); __builtin_amdgcn_s_barrier(); asm volatile("" ::: "memory"); } while (0)
#define VMWAIT(n) asm volatile("s_waitcnt vmcnt(" #n ")" ::: "memory")

__device__ __forceinline__ void dma16(const unsigned char* sbase, unsigned voff, unsigned lds) {
    unsigned keep;
    asm volatile("s_mov_b32 %0, m0\n\ts_mov_b32 m0, %3\n\ts_nop 0\n\tglobal_load_lds_dwordx4 %1, %2\n\ts_mov_b32 m0, %0"
                 : "=&s"(keep) : "v"(voff), "s"(sbase), "s"(lds) : "memory");
}
__device__ __forceinline__ v4u tr2(LAS unsigned char* p0, LAS unsigned char* p1) {
    typedef short v4i16_t __attribute__((ext_vector_type(4)));
    v4i16_t a = __builtin_amdgcn_ds_read_tr16_b64_v4i16((LAS v4i16_t*)p0);
    v4i16_t b = __builtin_amdgcn_ds_read_tr16_b64_v4i16((LAS v4i16_t*)p1);
    v2u x = __builtin_bit_cast(v2u, a), y = __builtin_bit_cast(v2u, b);
    return v4u{x.x, x.y, y.x, y.y};
}

__device__ __forceinline__ f32x4 gemv16(const v4u* wfr, LAS unsigned char* xb) {
    v4u f[8];
#pragma unroll
    for (int t = 0; t < 8; ++t) f[t] = *(LAS v4u*)(xb + t * 64);
    __builtin_amdgcn_sched_barrier(0);
    f32x4 acc = {0.f, 0.f, 0.f, 0.f};
#pragma unroll
    for (int t = 0; t < 8; ++t) { acc = mfma(wfr[t], f[t], acc); f[t] = *(LAS v4u*)(xb + (t + 8) * 64); __builtin_amdgcn_sched_barrier(0); }
#pragma unroll
    for (int t = 0; t < 8; ++t) acc = mfma(wfr[8 + t], f[t], acc);
    return acc;
}
#define STAMP(i) do {} while (0)

template <int T> struct Res { v4u wq[16], wo[16], kf[8], vf[8]; };

__global__ void __launch_bounds__(256, 1) mega(Params p) {
    extern __shared__ __attribute__((aligned(16))) unsigned char smem[];
    LAS unsigned char* L = (LAS unsigned char*)smem;
    const int c = blockIdx.x, tid = threadIdx.x, lane = tid & 63, w = __builtin_amdgcn_readfirstlane(tid >> 6);
    const int kvh = c & 7, s = (c >> 3) & 7, b = c >> 6, grp = b * 8 + kvh;
    const int g = lane >> 4, r16 = lane & 15, half = lane >> 5, i32 = lane & 31;
    const __amdgpu_buffer_rsrc_t rs = __builtin_amdgcn_make_buffer_rsrc(p.ws, 0, WS_END, 0x00020000);
#define LD4(off) __builtin_amdgcn_raw_buffer_load_b128(rs, (off), 0, AUX_SC1)
#define LD2(off) __builtin_amdgcn_raw_buffer_load_b64(rs, (off), 0, AUX_SC1)
#define LD1(off) __builtin_amdgcn_raw_buffer_load_b32(rs, (off), 0, AUX_SC1)
#define ST4(v, off) __builtin_amdgcn_raw_buffer_store_b128((v), rs, (off), 0, AUX_SC1)
#define ST2(v, off) __builtin_amdgcn_raw_buffer_store_b64((v), rs, (off), 0, AUX_SC1)
    STAMP(0);

    const unsigned wrow = lane >> 3, wpos = lane & 7;
    const unsigned wf = ((wrow >> 1) & 1);
    auto stage_w = [&](const u16* wbase, int ld_bytes, int unit_in_mat, int slot, int p0, int pstep) {
#pragma unroll 1
        for (int pc = p0; pc < 16; pc += pstep) {
            const unsigned f = wf | ((pc & 1) << 1);
            const unsigned ch = (((wpos >> 1) ^ f) << 1) | (wpos & 1);
            const unsigned voff = wrow * (unsigned)ld_bytes + ch * 16;
            const unsigned char* sb = (const unsigned char*)wbase + (size_t)(unit_in_mat * 128 + pc * 8) * ld_bytes;
            dma16(sb, voff, (unsigned)(L_STGW + slot * 16384 + pc * 1024));
        }
    };
    auto stage_v = [&](const u16* vbase) {
#pragma unroll
        for (int pc = 0; pc < 8; ++pc) {
            const unsigned row = pc * 4 + g;
            const unsigned ch = (((unsigned)(r16 >> 1) ^ (row & 7)) << 1) | (lane & 1);
            dma16((const unsigned char*)vbase + pc * 1024, (unsigned)(g * 256) + ch * 16, (unsigned)(L_STGV + w * 8192 + pc * 1024));
        }
    };
    const int q4 = r16 >> 2, p4 = lane & 3;
    const unsigned wtr = (unsigned)((8 * g + q4) * 128 + (((w ^ ((q4 >> 1) | ((g & 1) << 1))) << 1) + (p4 >> 1)) * 16 + (p4 & 1) * 8);
    auto consume_w = [&](v4u* dst, int slot) {
        LAS unsigned char* base = L + L_STGW + slot * 16384 + wtr;
#pragma unroll
        for (int t = 0; t < 4; ++t) dst[t] = tr2(base + t * 4096, base + t * 4096 + 512);
    };
    const unsigned vrow = 4 * g + q4;
    const unsigned vtr = (unsigned)(L_STGV + w * 8192) + vrow * 256 + (vrow & 7) * 32 + (p4 >> 1) * 16 + (p4 & 1) * 8;
    auto consume_v = [&](v4u* dst) {
#pragma unroll
        for (int t = 0; t < 8; ++t) { LAS unsigned char* a = L + (vtr ^ (unsigned)(t << 5)); dst[t] = tr2(a, a + 4096); }
    };
    auto load_k = [&](v4u* dst, const u16* kbase) {
        const GAS unsigned char* kb = (const GAS unsigned char*)kbase + r16 * 256 + g * 16;
#pragma unroll
        for (int i = 0; i < 2; ++i)
#pragma unroll
            for (int t = 0; t < 4; ++t) dst[i * 4 + t] = *(const GAS v4u*)(kb + i * 4096 + t * 64);
    };
    const size_t kvoff = ((size_t)(b * HKV + kvh) * SK + (SK - W) + s * 128 + w * 32) * DH;
    const u16* wqA = p.wq0 + kvh * 512 + s * 64; const u16* wqB = p.wq1 + kvh * 512 + s * 64;
    const u16* woA = p.wo0 + (size_t)kvh * 512 * 512 + s * 64; const u16* woB = p.wo1 + (size_t)kvh * 512 * 512 + s * 64;

    Res<0> RA; Res<1> RB;

    float x0, x1;
    {
        const v2u xv = *(const GAS v2u*)((const GAS unsigned char*)p.x + (b * 512 + 128 * w + 4 * i32) * 2);
        x0 = half ? bfhi(xv.x) : bflo(xv.x);
        x1 = half ? bfhi(xv.y) : bflo(xv.y);
    }
    unsigned long long gen1 = 0;
    if (w == 3) {
        const v2u gv = LD2(WS_GEN + b * 256);
        const v4u e = {EMPTY, EMPTY, EMPTY, EMPTY};
        if (lane < 32) ST4(e, WS_QB + (lane >> 3) * QB_L + grp * 1024 + s * 128 + (lane & 7) * 16);
#pragma unroll
        for (int l = 0; l < 4; ++l) {
            const int ob = WS_OB + l * OB_L + grp * OB_G + s * OB_P;
            ST4(e, ob + lane * 16);
            if (lane < 32) ST4(e, ob + 1024 + lane * 16);
            const int xb = WS_XB + l * XB_L + b * XB_B + (lane >> 4) * XB_R + kvh * 2048 + s * 256 + (lane & 15) * 16;
            ST4(e, xb); ST4(e, xb + 4 * XB_R);
        }
        VMWAIT(0);
        gen1 = (((unsigned long long)gv.y << 32) | gv.x) + 1ull;
        if (lane == 0) ST2((v2u{(unsigned)gen1, (unsigned)(gen1 >> 32)}), WS_HELLO + b * 512 + (c & 63) * 8);
    } else {
#pragma unroll 1
        for (int u = 0; u < 4; ++u) stage_w(wqA, 8192, u, u, w, 3);
    }
    ((LAS unsigned*)(L + L_ONES))[tid] = 0x3F803F80u;
    load_k(RA.kf, p.kc0 + kvoff);
    stage_v(p.vc0 + kvoff);
#pragma unroll 1
    for (int u = 0; u < 3; ++u) stage_w(woA, 1024, u, 4 + u, w, 4);

    auto gather_x = [&](const int l, const bool active) {
        const int off = WS_XB + l * XB_L + b * XB_B + kvh * XB_R + (half * 4) * 2048 + w * 512 + i32 * 16;
        v4u a0 = {0u, 0u, 0u, 0u}, a1 = a0, a2 = a0, a3 = a0;
        if (active) {
            for (unsigned sp = 0;; ++sp) { asm volatile("" ::: "memory");
                a0 = LD4(off); a1 = LD4(off + 2048); a2 = LD4(off + 4096); a3 = LD4(off + 6144);
                const unsigned m = max(max(mx4(a0), mx4(a1)), max(mx4(a2), mx4(a3)));
                if (__all(m != EMPTY) || sp > SPIN_CAP) break;
            }
        }
        float t[4];
#pragma unroll
        for (int j = 0; j < 4; ++j)
            t[j] = (__uint_as_float(a0[j]) + __uint_as_float(a1[j])) + (__uint_as_float(a2[j]) + __uint_as_float(a3[j]));
        const float o0 = swap32_add(t[0], t[1]);
        const float o1 = swap32_add(t[2], t[3]);
        x0 = rbf(x0 + rbf(o0));
        x1 = rbf(x1 + rbf(o1));
    };

    auto layer = [&](auto& R, auto TC, const int it) {
        constexpr int T = decltype(TC)::value;
        const int l = 2 * it + T;
        const bool first = (it == 0);
        if (T == 1 || !first) gather_x(l - 1, true);
        STAMP(1 + l * 8 + 0);
        {
            LAS u16* xs = (LAS u16*)(L + L_XS) + 128 * w + 4 * i32 + half;
            xs[0] = (u16)(__float_as_uint(x0) >> 16);
            xs[2] = (u16)(__float_as_uint(x1) >> 16);
        }
        {
            const float sh = __builtin_bit_cast(float, __builtin_amdgcn_readfirstlane(__builtin_bit_cast(int, x0)));
            const float d0 = x0 - sh, d1 = x1 - sh;
            float s1 = d0 + d1, s2 = d0 * d0 + d1 * d1;
            s1 = x16_sum(row16_sum(s1)); s2 = x16_sum(row16_sum(s2));
            const float mw = sh + s1 * (1.f / 128.f), m2 = s2 - s1 * s1 * (1.f / 128.f);
            if (lane == 0) *(LAS f32x2*)(L + L_ST + w * 8) = f32x2{mw, m2};
        }
        if (first) VMWAIT(0);
        BAR();
        if (first) {
            constexpr int s0 = T == 0 ? 0 : 1;
#pragma unroll
            for (int u = 0; u < 4; ++u) consume_w(R.wq + 4 * u, s0 + u);
        }
        f32x4 acc = {0.f, 0.f, 0.f, 0.f};
        {
            LAS unsigned char* xb = L + (r16 == 1 ? L_ONES : L_XS) + g * 16;
            acc = gemv16(R.wq, xb);
        }
        {
            const f32x4 st0 = *(LAS f32x4*)(L + L_ST), st1 = *(LAS f32x4*)(L + L_ST + 16);
            const float mean = 0.25f * ((st0[0] + st0[2]) + (st1[0] + st1[2]));
            const float e0 = st0[0] - mean, e1 = st0[2] - mean, e2 = st1[0] - mean, e3 = st1[2] - mean;
            const float var = ((st0[1] + st0[3]) + (st1[1] + st1[3]) + 128.f * ((e0 * e0 + e1 * e1) + (e2 * e2 + e3 * e3))) * (1.f / 512.f);
            const float rstd = __builtin_amdgcn_rsqf(var + 1e-5f);
            float qv[4];
#pragma unroll
            for (int j = 0; j < 4; ++j) qv[j] = (acc[j] - mean * dpp<SHL1>(acc[j])) * rstd;
            const v2u qo = {noempty(cvtpk(qv[0], qv[1])), noempty(cvtpk(qv[2], qv[3]))};
            if (r16 == 0) ST2(qo, WS_QB + l * QB_L + grp * 1024 + s * 128 + w * 32 + g * 8);
        }
        STAMP(1 + l * 8 + 1);
        if (first) {
            if (T == 0 && w == 3) {
                for (unsigned sp = 0;; ++sp) { asm volatile("" ::: "memory");
                    const v2u h = LD2(WS_HELLO + b * 512 + lane * 8);
                    const bool ok = h.x == (unsigned)gen1 && h.y == (unsigned)(gen1 >> 32);
                    if (__all(ok) || sp > SPIN_CAP) break;
                }
                const unsigned long long g2 = gen1;
                if ((c & 63) == 0 && lane == 0) ST2((v2u{(unsigned)g2, (unsigned)(g2 >> 32)}), WS_GEN + b * 256);
            }
            BAR();
            if (T == 0) {
                stage_w(woA, 1024, 3, 0, w, 4);
#pragma unroll 1
                for (int u = 0; u < 3; ++u) stage_w(wqB, 8192, u, 1 + u, w, 4);
            } else {
                stage_w(woB, 1024, 3, 1, w, 4);
            }
        }
        {
            const int off = WS_QB + l * QB_L + grp * 1024 + w * 256 + r16 * 16;
            v4u a0;
            for (unsigned sp = 0;; ++sp) { asm volatile("" ::: "memory");
                a0 = LD4(off);
                if (__all(mx4(a0) != EMPTY) || sp > SPIN_CAP) break;
            }
            if (lane < 16) *(LAS v4u*)(L + L_QS + w * 256 + r16 * 16) = a0;
        }
        STAMP(1 + l * 8 + 2);
        BAR();
        if (first) consume_v(R.vf);
        f32x4 sc0 = {0.f, 0.f, 0.f, 0.f}, sc1 = {0.f, 0.f, 0.f, 0.f};
        {
            LAS unsigned char* qb = L + L_QS + (lane & 3) * 256 + g * 16;
            v4u qf[4];
#pragma unroll
            for (int t = 0; t < 4; ++t) qf[t] = *(LAS v4u*)(qb + t * 64);
            __builtin_amdgcn_sched_barrier(0);
#pragma unroll
            for (int t = 0; t < 4; ++t) { sc0 = mfma(R.kf[t], qf[t], sc0); sc1 = mfma(R.kf[4 + t], qf[t], sc1); }
        }
        constexpr float CS = 0.08838834764831845f * 1.4426950408889634f;
        float mw = fmaxf(fmaxf(fmaxf(sc0[0], sc0[1]), fmaxf(sc0[2], sc0[3])), fmaxf(fmaxf(sc1[0], sc1[1]), fmaxf(sc1[2], sc1[3])));
        mw = x16_max(mw) * CS;
        float pe[8];
#pragma unroll
        for (int j = 0; j < 4; ++j) { pe[j] = __builtin_amdgcn_exp2f(sc0[j] * CS - mw); pe[4 + j] = __builtin_amdgcn_exp2f(sc1[j] * CS - mw); }
        const v4u pf = {cvtpk(pe[0], pe[1]), cvtpk(pe[2], pe[3]), cvtpk(pe[4], pe[5]), cvtpk(pe[6], pe[7])};
        f32x4 oacc[8];
#pragma unroll
        for (int t = 0; t < 8; ++t) oacc[t] = mfma(R.vf[t], pf, f32x4{0.f, 0.f, 0.f, 0.f});
        float lw = ((pe[0] + pe[1]) + (pe[2] + pe[3])) + ((pe[4] + pe[5]) + (pe[6] + pe[7]));
        lw = x16_sum(lw);
        if (r16 < 4) {
            LAS unsigned char* ow = L + L_OW + w * OW_W + r16 * OW_H + g * 16;
#pragma unroll
            for (int t = 0; t < 8; ++t) *(LAS f32x4*)(ow + t * 64) = oacc[t];
            if (g == 0) *(LAS f32x2*)(L + L_ML + r16 * 32 + w * 8) = f32x2{mw, lw};
        }
        if (first && T == 0) {
            asm volatile("s_waitcnt lgkmcnt(0)" ::: "memory");
            load_k(RB.kf, p.kc1 + kvoff);
            stage_v(p.vc1 + kvoff);
        }
        BAR();
        {
            const f32x4 ml0 = *(LAS f32x4*)(L + L_ML + w * 32), ml1 = *(LAS f32x4*)(L + L_ML + w * 32 + 16);
            LAS unsigned char* ow = L + L_OW + w * OW_H + i32 * 16;
            const f32x4 v0 = *(LAS f32x4*)(ow), v1 = *(LAS f32x4*)(ow + OW_W), v2 = *(LAS f32x4*)(ow + 2 * OW_W), v3 = *(LAS f32x4*)(ow + 3 * OW_W);
            const float M = fmaxf(fmaxf(ml0[0], ml0[2]), fmaxf(ml1[0], ml1[2]));
            const float a0 = __builtin_amdgcn_exp2f(ml0[0] - M), a1 = __builtin_amdgcn_exp2f(ml0[2] - M), a2 = __builtin_amdgcn_exp2f(ml1[0] - M), a3 = __builtin_amdgcn_exp2f(ml1[2] - M);
            const float Ls = (a0 * ml0[1] + a1 * ml0[3]) + (a2 * ml1[1] + a3 * ml1[3]);
            const float inv = __builtin_amdgcn_rcpf(Ls);
            const float b0 = a0 * inv, b1 = a1 * inv, b2 = a2 * inv, b3 = a3 * inv;
            float o[4];
#pragma unroll
            for (int j = 0; j < 4; ++j) o[j] = (b0 * v0[j] + b1 * v1[j]) + (b2 * v2[j] + b3 * v3[j]);
            v2u ov = {noempty(cvtpk(o[0], o[1])), noempty(cvtpk(o[2], o[3]))};
            const float lam = M + __builtin_amdgcn_logf(Ls);
            if (lane == 32) ov = v2u{noempty(__float_as_uint(lam)), 0u};
            if (lane <= 32) ST2(ov, WS_OB + l * OB_L + grp * OB_G + s * OB_P + w * 384 + lane * 8);
        }
        STAMP(1 + l * 8 + 3);
        {
            const int off = WS_OB + l * OB_L + grp * OB_G + g * OB_P + w * 384 + r16 * 16;
            const int offl = WS_OB + l * OB_L + grp * OB_G + g * OB_P + w * 384 + 256;
            v4u a0, a1; unsigned la, lb;
            for (unsigned sp = 0;; ++sp) { asm volatile("" ::: "memory");
                a0 = LD4(off); a1 = LD4(off + 4 * OB_P); la = LD1(offl); lb = LD1(offl + 4 * OB_P);
                const unsigned m = max(max(mx4(a0), mx4(a1)), max(la, lb));
                if (__all(m != EMPTY) || sp > SPIN_CAP) break;
            }
            STAMP(1 + l * 8 + 4);
            const float fa = __uint_as_float(la), fb = __uint_as_float(lb);
            const float M = x16_max(fmaxf(fa, fb));
            float ea = __builtin_amdgcn_exp2f(fa - M), eb = __builtin_amdgcn_exp2f(fb - M);
            const float inv = __builtin_amdgcn_rcpf(x16_sum(ea + eb));
            ea *= inv; eb *= inv;
            float v[8];
#pragma unroll
            for (int j = 0; j < 4; ++j) { v[2 * j] = ea * bflo(a0[j]) + eb * bflo(a1[j]); v[2 * j + 1] = ea * bfhi(a0[j]) + eb * bfhi(a1[j]); }
            float u[4];
#pragma unroll
            for (int j = 0; j < 4; ++j) u[j] = swap32_add(v[j], v[j + 4]);
            const float z0 = swap16_add(u[0], u[2]), z1 = swap16_add(u[1], u[3]);
            *(LAS unsigned*)(L + L_OS + (w * 128 + 8 * r16 + 4 * half + 2 * (g & 1)) * 2) = cvtpk(z0, z1);
        }
        if (first) VMWAIT(0);
        BAR();
        if (first) {
            if (T == 0) { consume_w(R.wo, 4); consume_w(R.wo + 4, 5); consume_w(R.wo + 8, 6); consume_w(R.wo + 12, 0); }
            else { consume_w(R.wo, 5); consume_w(R.wo + 4, 6); consume_w(R.wo + 8, 0); consume_w(R.wo + 12, 1); }
        }
        f32x4 oa = {0.f, 0.f, 0.f, 0.f};
        {
            LAS unsigned char* ob = L + L_OS + g * 16;
            oa = gemv16(R.wo, ob);
        }
        {
            const v4u ov = {noempty(__float_as_uint(oa[0])), noempty(__float_as_uint(oa[1])), noempty(__float_as_uint(oa[2])), noempty(__float_as_uint(oa[3]))};
            if (r16 < 8) ST4(ov, WS_XB + l * XB_L + b * XB_B + r16 * XB_R + kvh * 2048 + s * 256 + w * 64 + g * 16);
        }
        STAMP(1 + l * 8 + 5);
        if (first && T == 0) {
            BAR();
            stage_w(wqB, 8192, 3, 4, w, 4);
            stage_w(woB, 1024, 0, 5, w, 4);
            stage_w(woB, 1024, 1, 6, w, 4);
            stage_w(woB, 1024, 2, 0, w, 4);
        }
    };

#pragma unroll 1
    for (int it = 0; it < 2; ++it) {
        layer(RA, std::integral_constant<int, 0>{}, it);
        layer(RB, std::integral_constant<int, 1>{}, it);
    }
    if (s == 0 && w == (kvh >> 1)) {
        const bool act = (i32 >> 4) == (kvh & 1);
        gather_x(3, act);
        if (act) {
            GAS u16* o = (GAS u16*)p.out + b * 512 + 128 * w + 4 * i32 + half;
            o[0] = (u16)(__float_as_uint(x0) >> 16);
            o[2] = (u16)(__float_as_uint(x1) >> 16);
        }
    }
    STAMP(40);
}
}

extern "C" void kernel_launch(void* const* d_in, const int* in_sizes, int n_in, void* d_out, int out_size, void* d_ws, size_t ws_size, hipStream_t stream) {
    mk::Params p;
    p.x = (const u16*)d_in[0]; p.kc0 = (const u16*)d_in[1]; p.vc0 = (const u16*)d_in[2]; p.wq0 = (const u16*)d_in[3]; p.wo0 = (const u16*)d_in[4];
    p.kc1 = (const u16*)d_in[6]; p.vc1 = (const u16*)d_in[7]; p.wq1 = (const u16*)d_in[8]; p.wo1 = (const u16*)d_in[9];
    p.out = (u16*)d_out; p.ws = (unsigned char*)d_ws;
    (void)hipFuncSetAttribute((const void*)mk::mega, hipFuncAttributeMaxDynamicSharedMemorySize, mk::LDS_BYTES);
    hipLaunchKernelGGL(mk::mega, dim3(256), dim3(256), mk::LDS_BYTES, stream, p);
}
```

```cpp
#include <hip/hip_runtime.h>
#include <hip/hip_bf16.h>
#include <stdint.h>
#include <type_traits>

#define MK_STAMPS 0
#define NLAY 4
#define DRYRUN 0
#define PACE 63
#define CODE_KB 10
#define PRETOUCH 1
#define DLY_Q 6
#define DLY_O 6
#define DLY_X 14

#define GAS __attribute__((address_space(1)))
#define LAS __attribute__((address_space(3)))
typedef unsigned short u16;
typedef unsigned v4u __attribute__((ext_vector_type(4)));
typedef unsigned v2u __attribute__((ext_vector_type(2)));
typedef float f32x4 __attribute__((ext_vector_type(4)));
typedef float f32x2 __attribute__((ext_vector_type(2)));
typedef __bf16 bf16x8 __attribute__((ext_vector_type(8)));
typedef __bf16 bf16x2 __attribute__((ext_vector_type(2)));

namespace mk {
constexpr int HKV = 8, SK = 4096, DH = 128, W = 1024;
constexpr unsigned EMPTY = 0xFFFFFFFFu;
constexpr int AUX_SC1 = 16;
constexpr unsigned SPIN_CAP = 1u << 17;

constexpr int WS_GEN = 0;
constexpr int WS_HELLO = 1024;
constexpr int WS_QT = 4096;
constexpr int WS_QB = WS_QT + 32 * 2048;
constexpr int QB_L = 32 * 1024;
constexpr int WS_OB = WS_QB + NLAY * QB_L;
constexpr int OB_P = 1024, OB_G = 8 * OB_P, OB_L = 32 * OB_G;
constexpr int WS_LB = WS_OB + NLAY * OB_L;
constexpr int LB_L = 32 * 128;
constexpr int WS_XB = WS_LB + NLAY * LB_L;
constexpr int XB_K = 1024, XB_R = 8 * XB_K, XB_B = 8 * XB_R, XB_L = 4 * XB_B;
constexpr int WS_END = WS_XB + NLAY * XB_L;
constexpr int WS_STAMP = (WS_END + 4095) & ~4095;

constexpr int RW = 36864;
constexpr int U0 = 0, U1 = 16384;
constexpr int L_EX = 4 * RW;
constexpr int L_XS = L_EX;
constexpr int L_QS = L_XS + 1024;
constexpr int L_PS = L_QS + 1024;
constexpr int L_ON = L_PS + 1024;
constexpr int L_RQ = L_ON + 256;
constexpr int L_RC = L_RQ + 1024;
constexpr int L_RX = L_RC + 1024;
constexpr int L_ML = L_RX + 1024;
constexpr int L_ST = L_ML + 128;
constexpr int L_GEN = L_ST + 128;
constexpr int L_WT = L_GEN + 16;
constexpr int L_TRASH = L_WT + 128;
constexpr int L_GENB = L_TRASH + 1024;
constexpr int L_END = L_GENB + 1024;
constexpr int LDS_BYTES = 160 * 1024;
static_assert(L_END <= LDS_BYTES, "lds");
static_assert((L_WT % 16) == 0 && (L_ST % 16) == 0 && (L_RQ % 16) == 0 && (L_TRASH % 16) == 0, "align");

struct Params {
    const u16* x; const u16* kc0; const u16* vc0; const u16* wq0; const u16* wo0;
    const u16* kc1; const u16* vc1; const u16* wq1; const u16* wo1;
    u16* out; unsigned char* ws;
};

__device__ __forceinline__ f32x4 mfma(v4u a, v4u b, f32x4 c) {
    return __builtin_amdgcn_mfma_f32_16x16x32_bf16(__builtin_bit_cast(bf16x8, a), __builtin_bit_cast(bf16x8, b), c, 0, 0, 0);
}
__device__ __forceinline__ unsigned cvtpk(float lo, float hi) {
    bf16x2 r = {(__bf16)lo, (__bf16)hi};
    return __builtin_bit_cast(unsigned, r);
}
__device__ __forceinline__ float bflo(unsigned u) { return __builtin_bit_cast(float, u << 16); }
__device__ __forceinline__ float bfhi(unsigned u) { return __builtin_bit_cast(float, u & 0xFFFF0000u); }
template <int CTRL> __device__ __forceinline__ float dpp(float x) {
    return __builtin_bit_cast(float, __builtin_amdgcn_mov_dpp(__builtin_bit_cast(int, x), CTRL, 0xf, 0xf, true));
}
constexpr int XOR1 = 0xB1, XOR2 = 0x4E, HMIR = 0x141, MIR = 0x140, SHL1 = 0x101;
__device__ __forceinline__ float x16_max(float x) {
    auto s = __builtin_amdgcn_permlane16_swap(__float_as_uint(x), __float_as_uint(x), false, false);
    x = fmaxf(__uint_as_float(s[0]), __uint_as_float(s[1]));
    auto t = __builtin_amdgcn_permlane32_swap(__float_as_uint(x), __float_as_uint(x), false, false);
    return fmaxf(__uint_as_float(t[0]), __uint_as_float(t[1]));
}
__device__ __forceinline__ float x16_sum(float x) {
    auto s = __builtin_amdgcn_permlane16_swap(__float_as_uint(x), __float_as_uint(x), false, false);
    x = __uint_as_float(s[0]) + __uint_as_float(s[1]);
    auto t = __builtin_amdgcn_permlane32_swap(__float_as_uint(x), __float_as_uint(x), false, false);
    return __uint_as_float(t[0]) + __uint_as_float(t[1]);
}
__device__ __forceinline__ float row16_sum(float x) { x += dpp<XOR1>(x); x += dpp<XOR2>(x); x += dpp<HMIR>(x); x += dpp<MIR>(x); return x; }
__device__ __forceinline__ float row8_sum(float x) { x += dpp<XOR1>(x); x += dpp<XOR2>(x); x += dpp<HMIR>(x); return x; }
__device__ __forceinline__ float row8_max(float x) { x = fmaxf(x, dpp<XOR1>(x)); x = fmaxf(x, dpp<XOR2>(x)); x = fmaxf(x, dpp<HMIR>(x)); return x; }
__device__ __forceinline__ float swap32_add(float a, float b) {
    auto s = __builtin_amdgcn_permlane32_swap(__float_as_uint(a), __float_as_uint(b), false, false);
    return __uint_as_float(s[0]) + __uint_as_float(s[1]);
}
__device__ __forceinline__ float swap16_add(float a, float b) {
    auto s = __builtin_amdgcn_permlane16_swap(__float_as_uint(a), __float_as_uint(b), false, false);
    return __uint_as_float(s[0]) + __uint_as_float(s[1]);
}
__device__ __forceinline__ unsigned mx4(v4u v) { return max(max(v.x, v.y), max(v.z, v.w)); }
__device__ __forceinline__ unsigned noempty(unsigned u) { return min(u, 0xFFFFFFFEu); }
__device__ __forceinline__ f32x2 lo2(v4u v) { return f32x2{__uint_as_float(v.x), __uint_as_float(v.y)}; }
__device__ __forceinline__ f32x2 hi2(v4u v) { return f32x2{__uint_as_float(v.z), __uint_as_float(v.w)}; }

#define SB() __builtin_amdgcn_sched_barrier(0)
#define KEEP_ACC() asm volatile("" ::: "a0", "a1", "a2", "a3", "a4", "a5", "a6", "a7", "a8", "a9", "a10", "a11", "a12", "a13", "a14", "a15", "a16", "a17", "a18", "a19", "a20", "a21", "a22", "a23", "a24", "a25", "a26", "a27", "a28", "a29", "a30", "a31", "a32", "a33", "a34", "a35", "a36", "a37", "a38", "a39", "a40", "a41", "a42", "a43", "a44", "a45", "a46", "a47", "a48", "a49", "a50", "a51", "a52", "a53", "a54", "a55", "a56", "a57", "a58", "a59", "a60", "a61", "a62", "a63", "a64", "a65", "a66", "a67", "a68", "a69", "a70", "a71", "a72", "a73", "a74", "a75", "a76", "a77", "a78", "a79", "a80", "a81", "a82", "a83", "a84", "a85", "a86", "a87", "a88", "a89", "a90", "a91", "a92", "a93", "a94", "a95", "a96", "a97", "a98", "a99", "a100", "a101", "a102", "a103", "a104", "a105", "a106", "a107", "a108", "a109", "a110", "a111", "a112", "a113", "a114", "a115", "a116", "a117", "a118", "a119", "a120", "a121", "a122", "a123", "a124", "a125", "a126", "a127", "a128", "a129", "a130", "a131", "a132", "a133", "a134", "a135", "a136", "a137", "a138", "a139", "a140", "a141", "a142", "a143", "a144", "a145", "a146", "a147", "a148", "a149", "a150", "a151", "a152", "a153", "a154", "a155", "a156", "a157", "a158", "a159", "a160", "a161", "a162", "a163", "a164", "a165", "a166", "a167", "a168", "a169", "a170", "a171", "a172", "a173", "a174", "a175", "a176", "a177", "a178", "a179", "a180", "a181", "a182", "a183", "a184", "a185", "a186", "a187", "a188", "a189", "a190", "a191", "a192", "a193", "a194", "a195", "a196", "a197", "a198", "a199", "a200", "a201", "a202", "a203", "a204", "a205", "a206", "a207", "a208", "a209", "a210", "a211", "a212", "a213", "a214", "a215", "a216", "a217", "a218", "a219", "a220", "a221", "a222", "a223", "a224", "a225", "a226", "a227", "a228", "a229", "a230", "a231", "a232", "a233", "a234", "a235", "a236", "a237", "a238", "a239", "a240", "a241", "a242", "a243", "a244", "a245", "a246", "a247", "a248", "a249", "a250", "a251", "a252", "a253", "a254", "a255")
#define MFMA0(acc, A, B, CA) asm volatile("v_mfma_f32_16x16x32_bf16 %0, %1, %2, 0" : "=&v"(acc) : CA(A), "v"(B))
#define MFMA(acc, A, B, CA) asm volatile("v_mfma_f32_16x16x32_bf16 %0, %1, %2, %0" : "+v"(acc) : CA(A), "v"(B))
#define PIN(x) asm volatile("" : "+v"(x))
#define MFMAW0(acc, A, N) asm volatile("v_mfma_f32_16x16x32_bf16 %0, %1, a[%c2:%c3], 0" : "=&v"(acc) : "v"(A), "i"(N), "i"((N) + 3))
#define MFMAW(acc, A, N) asm volatile("v_mfma_f32_16x16x32_bf16 %0, %1, a[%c2:%c3], %0" : "+v"(acc) : "v"(A), "i"(N), "i"((N) + 3))
template <int N> __device__ __forceinline__ void tr_col(unsigned addr) {
    asm volatile("ds_read_b64_tr_b16 a[%c1:%c2], %0\n\tds_read_b64_tr_b16 a[%c3:%c4], %0 offset:512\n\t"
                 "ds_read_b64_tr_b16 a[%c5:%c6], %0 offset:4096\n\tds_read_b64_tr_b16 a[%c7:%c8], %0 offset:4608"
                 :: "v"(addr), "i"(N), "i"(N + 1), "i"(N + 2), "i"(N + 3), "i"(N + 4), "i"(N + 5), "i"(N + 6), "i"(N + 7) : "memory");
    asm volatile("ds_read_b64_tr_b16 a[%c1:%c2], %0 offset:8192\n\tds_read_b64_tr_b16 a[%c3:%c4], %0 offset:8704\n\t"
                 "ds_read_b64_tr_b16 a[%c5:%c6], %0 offset:12288\n\tds_read_b64_tr_b16 a[%c7:%c8], %0 offset:12800"
                 :: "v"(addr), "i"(N + 8), "i"(N + 9), "i"(N + 10), "i"(N + 11), "i"(N + 12), "i"(N + 13), "i"(N + 14), "i"(N + 15) : "memory");
}
#define QBLOCK(NB) asm volatile("v_mfma_f32_16x16x32_bf16 %[c0], %[f0], a[%c[N]+0:%c[N]+3], 0\n\t" \
        "v_mfma_f32_16x16x32_bf16 %[c1], %[f0], a[%c[N]+16:%c[N]+19], 0\n\t" \
        "s_waitcnt lgkmcnt(0)\n\t" \
        "s_cmp_ge_i32 %[live], 0\n\t" \
        "s_cbranch_scc0 1f\n\t" \
        "s_barrier\n\t" \
        "1:\n\t" \
        "ds_read_b32 %[p1], %[ast]\n\t" \
        "ds_read_b32 %[p2], %[ast] offset:4\n\t" \
        "v_mfma_f32_16x16x32_bf16 %[c2], %[f0], a[%c[N]+32:%c[N]+35], 0\n\t" \
        "v_mfma_f32_16x16x32_bf16 %[c3], %[f0], a[%c[N]+48:%c[N]+51], 0\n\t" \
        "v_mfma_f32_16x16x32_bf16 %[c0], %[f1], a[%c[N]+4:%c[N]+7], %[c0]\n\t" \
        "v_mfma_f32_16x16x32_bf16 %[c1], %[f1], a[%c[N]+20:%c[N]+23], %[c1]\n\t" \
        "s_waitcnt lgkmcnt(0)\n\t" \
        "v_add_f32_dpp %[p1], %[p1], %[p1] quad_perm:[1,0,3,2] row_mask:0xf bank_mask:0xf\n\t" \
        "v_add_f32_dpp %[p2], %[p2], %[p2] quad_perm:[1,0,3,2] row_mask:0xf bank_mask:0xf\n\t" \
        "v_mfma_f32_16x16x32_bf16 %[c2], %[f1], a[%c[N]+36:%c[N]+39], %[c2]\n\t" \
        "v_add_f32_dpp %[p1], %[p1], %[p1] quad_perm:[2,3,0,1] row_mask:0xf bank_mask:0xf\n\t" \
        "v_add_f32_dpp %[p2], %[p2], %[p2] quad_perm:[2,3,0,1] row_mask:0xf bank_mask:0xf\n\t" \
        "v_mfma_f32_16x16x32_bf16 %[c3], %[f1], a[%c[N]+52:%c[N]+55], %[c3]\n\t" \
        "v_add_f32_dpp %[p1], %[p1], %[p1] row_half_mirror row_mask:0xf bank_mask:0xf\n\t" \
        "v_add_f32_dpp %[p2], %[p2], %[p2] row_half_mirror row_mask:0xf bank_mask:0xf\n\t" \
        "v_mfma_f32_16x16x32_bf16 %[c0], %[f2], a[%c[N]+8:%c[N]+11], %[c0]\n\t" \
        "v_add_f32_dpp %[p1], %[p1], %[p1] row_mirror row_mask:0xf bank_mask:0xf\n\t" \
        "v_add_f32_dpp %[p2], %[p2], %[p2] row_mirror row_mask:0xf bank_mask:0xf\n\t" \
        "v_mfma_f32_16x16x32_bf16 %[c1], %[f2], a[%c[N]+24:%c[N]+27], %[c1]\n\t" \
        "v_mul_f32 %[p1], 0x3b000000, %[p1]\n\t" \
        "v_mul_f32 %[p2], 0x3b000000, %[p2]\n\t" \
        "v_mfma_f32_16x16x32_bf16 %[c2], %[f2], a[%c[N]+40:%c[N]+43], %[c2]\n\t" \
        "v_fma_f32 %[p2], -%[p1], %[p1], %[p2]\n\t" \
        "v_max_f32 %[p2], 0, %[p2]\n\t" \
        "v_mfma_f32_16x16x32_bf16 %[c3], %[f2], a[%c[N]+56:%c[N]+59], %[c3]\n\t" \
        "v_add_f32 %[p2], 0x3727c5ac, %[p2]\n\t" \
        "v_rsq_f32 %[p2], %[p2]\n\t" \
        "v_mfma_f32_16x16x32_bf16 %[c0], %[f3], a[%c[N]+12:%c[N]+15], %[c0]\n\t" \
        "v_mfma_f32_16x16x32_bf16 %[c1], %[f3], a[%c[N]+28:%c[N]+31], %[c1]\n\t" \
        "v_mul_f32 %[p1], %[p1], %[p2]\n\t" \
        "v_sub_f32 %[p1], 0, %[p1]\n\t" \
        "v_mfma_f32_16x16x32_bf16 %[c2], %[f3], a[%c[N]+44:%c[N]+47], %[c2]\n\t" \
        "v_mfma_f32_16x16x32_bf16 %[c3], %[f3], a[%c[N]+60:%c[N]+63], %[c3]\n\t" \
        "s_nop 7\n\t" "" \
        : [c0] "=&v"(acc[0]), [c1] "=&v"(acc[1]), [c2] "=&v"(acc[2]), [c3] "=&v"(acc[3]), [p1] "=&v"(tq), [p2] "=&v"(rstd) \
        : [f0] "v"(f[0]), [f1] "v"(f[1]), [f2] "v"(f[2]), [f3] "v"(f[3]), [ast] "v"(a_st), [live] "s"(livei), [N] "i"(NB) : "memory", "scc")
#define QKBLOCK(R) asm volatile("v_mfma_f32_16x16x32_bf16 %[s0], %[k0], %[q0], 0\n\t" \
        "v_mfma_f32_16x16x32_bf16 %[s1], %[k4], %[q0], 0\n\t" \
        "v_mfma_f32_16x16x32_bf16 %[s0], %[k1], %[q1], %[s0]\n\t" \
        "v_mfma_f32_16x16x32_bf16 %[s1], %[k5], %[q1], %[s1]\n\t" \
        "v_mfma_f32_16x16x32_bf16 %[s0], %[k2], %[q2], %[s0]\n\t" \
        "v_mfma_f32_16x16x32_bf16 %[s1], %[k6], %[q2], %[s1]\n\t" \
        "v_mfma_f32_16x16x32_bf16 %[s0], %[k3], %[q3], %[s0]\n\t" \
        "v_mfma_f32_16x16x32_bf16 %[s1], %[k7], %[q3], %[s1]\n\t" "" \
        : [s0] "=&v"(sc0), [s1] "=&v"(sc1) \
        : [k0] "v"(R.kf[0]), [k1] "v"(R.kf[1]), [k2] "v"(R.kf[2]), [k3] "v"(R.kf[3]), [k4] "v"(R.kf[4]), [k5] "v"(R.kf[5]), [k6] "v"(R.kf[6]), [k7] "v"(R.kf[7]), [q0] "v"(qf[0]), [q1] "v"(qf[1]), [q2] "v"(qf[2]), [q3] "v"(qf[3]))
#define PVBLOCK(R) asm volatile("v_mfma_f32_16x16x32_bf16 %[o0], %[v0], %[p0], 0\n\t" \
        "v_mfma_f32_16x16x32_bf16 %[o1], %[v1], %[p0], 0\n\t" \
        "v_mfma_f32_16x16x32_bf16 %[o2], %[v2], %[p1], 0\n\t" \
        "v_mfma_f32_16x16x32_bf16 %[o3], %[v3], %[p1], 0\n\t" \
        "v_mfma_f32_16x16x32_bf16 %[o4], %[v4], %[p2], 0\n\t" \
        "v_mfma_f32_16x16x32_bf16 %[o5], %[v5], %[p2], 0\n\t" \
        "v_mfma_f32_16x16x32_bf16 %[o6], %[v6], %[p3], 0\n\t" \
        "v_mfma_f32_16x16x32_bf16 %[o7], %[v7], %[p3], 0\n\t" \
        "s_nop 7\n\t" "" \
        : [o0] "=&v"(oa[0][0]), [o1] "=&v"(oa[0][1]), [o2] "=&v"(oa[1][0]), [o3] "=&v"(oa[1][1]), [o4] "=&v"(oa[2][0]), [o5] "=&v"(oa[2][1]), [o6] "=&v"(oa[3][0]), [o7] "=&v"(oa[3][1]) \
        : [v0] "v"(R.vf[0]), [v1] "v"(R.vf[1]), [v2] "v"(R.vf[2]), [v3] "v"(R.vf[3]), [v4] "v"(R.vf[4]), [v5] "v"(R.vf[5]), [v6] "v"(R.vf[6]), [v7] "v"(R.vf[7]), [p0] "v"(pf[0]), [p1] "v"(pf[1]), [p2] "v"(pf[2]), [p3] "v"(pf[3]))
#define OBLOCK(NB) asm volatile("v_mfma_f32_16x16x32_bf16 %[L0], %[a0], a[%c[N]+0:%c[N]+3], 0\n\t" \
        "v_max_f32_dpp %[m], %[la], %[la] quad_perm:[1,0,3,2] row_mask:0xf bank_mask:0xf\n\t" \
        "v_mfma_f32_16x16x32_bf16 %[H0], %[a0], a[%c[N]+4:%c[N]+7], 0\n\t" \
        "s_nop 0\n\t" \
        "v_max_f32_dpp %[m], %[m], %[m] quad_perm:[2,3,0,1] row_mask:0xf bank_mask:0xf\n\t" \
        "v_mfma_f32_16x16x32_bf16 %[L1], %[a0], a[%c[N]+16:%c[N]+19], 0\n\t" \
        "s_nop 0\n\t" \
        "v_max_f32_dpp %[m], %[m], %[m] row_half_mirror row_mask:0xf bank_mask:0xf\n\t" \
        "v_mfma_f32_16x16x32_bf16 %[H1], %[a0], a[%c[N]+20:%c[N]+23], 0\n\t" \
        "v_sub_f32 %[m], %[la], %[m]\n\t" \
        "v_exp_f32 %[e], %[m]\n\t" \
        "v_mfma_f32_16x16x32_bf16 %[L2], %[a0], a[%c[N]+32:%c[N]+35], 0\n\t" \
        "s_nop 0\n\t" \
        "v_add_f32_dpp %[m], %[e], %[e] quad_perm:[1,0,3,2] row_mask:0xf bank_mask:0xf\n\t" \
        "v_mfma_f32_16x16x32_bf16 %[H2], %[a0], a[%c[N]+36:%c[N]+39], 0\n\t" \
        "s_nop 0\n\t" \
        "v_add_f32_dpp %[m], %[m], %[m] quad_perm:[2,3,0,1] row_mask:0xf bank_mask:0xf\n\t" \
        "v_mfma_f32_16x16x32_bf16 %[L3], %[a0], a[%c[N]+48:%c[N]+51], 0\n\t" \
        "s_nop 0\n\t" \
        "v_add_f32_dpp %[m], %[m], %[m] row_half_mirror row_mask:0xf bank_mask:0xf\n\t" \
        "v_mfma_f32_16x16x32_bf16 %[H3], %[a0], a[%c[N]+52:%c[N]+55], 0\n\t" \
        "v_rcp_f32 %[m], %[m]\n\t" \
        "v_mfma_f32_16x16x32_bf16 %[L0], %[a1], a[%c[N]+8:%c[N]+11], %[L0]\n\t" \
        "v_mul_f32 %[e], %[e], %[m]\n\t" \
        "ds_write_b32 %[aw], %[e]\n\t" \
        "v_mfma_f32_16x16x32_bf16 %[H0], %[a1], a[%c[N]+12:%c[N]+15], %[H0]\n\t" \
        "ds_read_b128 %[wt], %[ar]\n\t" \
        "v_mfma_f32_16x16x32_bf16 %[L1], %[a1], a[%c[N]+24:%c[N]+27], %[L1]\n\t" \
        "v_mfma_f32_16x16x32_bf16 %[H1], %[a1], a[%c[N]+28:%c[N]+31], %[H1]\n\t" \
        "v_mfma_f32_16x16x32_bf16 %[L2], %[a1], a[%c[N]+40:%c[N]+43], %[L2]\n\t" \
        "v_mfma_f32_16x16x32_bf16 %[H2], %[a1], a[%c[N]+44:%c[N]+47], %[H2]\n\t" \
        "v_mfma_f32_16x16x32_bf16 %[L3], %[a1], a[%c[N]+56:%c[N]+59], %[L3]\n\t" \
        "v_mfma_f32_16x16x32_bf16 %[H3], %[a1], a[%c[N]+60:%c[N]+63], %[H3]\n\t" \
        "s_waitcnt lgkmcnt(0)\n\t" \
        "s_nop 6\n\t" "" \
        : [L0] "=&v"(aL[0]), [H0] "=&v"(aH[0]), [L1] "=&v"(aL[1]), [H1] "=&v"(aH[1]), [L2] "=&v"(aL[2]), [H2] "=&v"(aH[2]), [L3] "=&v"(aL[3]), [H3] "=&v"(aH[3]), \
          [wt] "=&v"(wt), [m] "=&v"(tm), [e] "=&v"(te) \
        : [a0] "v"(oa0), [a1] "v"(oa1), [la] "v"(la), [aw] "v"(a_wtw), [ar] "v"(a_wtr), [N] "i"(NB) : "memory")
__device__ __forceinline__ float max3f(float a, float b, float c) { float r; asm("v_max3_f32 %0, %1, %2, %3" : "=v"(r) : "v"(a), "v"(b), "v"(c)); return r; }
__device__ __forceinline__ float max2f(float a, float b) { float r; asm("v_max_f32 %0, %1, %2" : "=v"(r) : "v"(a), "v"(b)); return r; }
__device__ __forceinline__ float x16_maxf(float x) {
    auto s = __builtin_amdgcn_permlane16_swap(__float_as_uint(x), __float_as_uint(x), false, false);
    x = max2f(__uint_as_float(s[0]), __uint_as_float(s[1]));
    auto t = __builtin_amdgcn_permlane32_swap(__float_as_uint(x), __float_as_uint(x), false, false);
    return max2f(__uint_as_float(t[0]), __uint_as_float(t[1]));
}
constexpr unsigned OOB = 0x80000000u;
template <int CTRL> __device__ __forceinline__ void dpp_acc(float& x) { x += dpp<CTRL>(x); asm("" : "+v"(x)); }
template <int CTRL> __device__ __forceinline__ void dpp_mx(float& x) { x = max2f(x, dpp<CTRL>(x)); }
#define UNLIKELY(x) __builtin_expect(!!(x), 0)
#define BAR() do { asm volatile("s_waitcnt lgkmcnt(0)" ::: "memory"); __builtin_amdgcn_s_barrier(); asm volatile("" ::: "memory"); } while (0)
#define VMWAIT(n) asm volatile("s_waitcnt vmcnt(" #n ")" ::: "memory")
template <int N> __device__ __forceinline__ void delay() {
#pragma unroll
    for (int i = 0; i < N; ++i) asm volatile("s_nop 15");
}

__device__ __forceinline__ void dma16(const unsigned char* sbase, unsigned voff, unsigned lds) {
    unsigned keep;
    asm volatile("s_mov_b32 %0, m0\n\ts_mov_b32 m0, %3\n\ts_nop 2\n\tglobal_load_lds_dwordx4 %1, %2 nt\n\ts_mov_b32 m0, %0\n\ts_waitcnt vmcnt(%c4)"
                 : "=&s"(keep) : "v"(voff), "s"(sbase), "s"(lds), "i"(PACE - 1) : "memory");
}
__device__ __forceinline__ void dma16c(const unsigned char* sbase, unsigned voff, unsigned lds) {
    unsigned keep;
    asm volatile("s_mov_b32 %0, m0\n\ts_mov_b32 m0, %3\n\ts_nop 2\n\tglobal_load_lds_dwordx4 %1, %2\n\ts_mov_b32 m0, %0"
                 : "=&s"(keep) : "v"(voff), "s"(sbase), "s"(lds) : "memory");
}
__device__ __forceinline__ void dma16s(const unsigned char* sbase, unsigned voff, unsigned lds) {
    unsigned keep;
    asm volatile("s_mov_b32 %0, m0\n\ts_mov_b32 m0, %3\n\ts_nop 2\n\tglobal_load_lds_dwordx4 %1, %2 sc1\n\ts_mov_b32 m0, %0"
                 : "=&s"(keep) : "v"(voff), "s"(sbase), "s"(lds) : "memory");
}
__device__ __forceinline__ void dma16x4(const unsigned char* sbase, unsigned v0, unsigned v1, unsigned v2, unsigned v3, unsigned lds) {
    unsigned keep;
    asm volatile("s_mov_b32 %0, m0\n\ts_mov_b32 m0, %6\n\ts_nop 2\n\t"
                 "global_load_lds_dwordx4 %1, %5 nt\n\ts_waitcnt vmcnt(%c7)\n\tglobal_load_lds_dwordx4 %2, %5 offset:1024 nt\n\ts_waitcnt vmcnt(%c7)\n\t"
                 "global_load_lds_dwordx4 %3, %5 offset:2048 nt\n\ts_waitcnt vmcnt(%c7)\n\tglobal_load_lds_dwordx4 %4, %5 offset:3072 nt\n\ts_waitcnt vmcnt(%c7)\n\ts_mov_b32 m0, %0"
                 : "=&s"(keep) : "v"(v0), "v"(v1), "v"(v2), "v"(v3), "s"(sbase), "s"(lds), "i"(PACE - 1) : "memory");
}
template <class T> __device__ __forceinline__ T opq_s(T v) { asm volatile("" : "+s"(v)); return v; }
template <class T> __device__ __forceinline__ T opq_v(T v) { asm volatile("" : "+v"(v)); return v; }
#define BLD4(dst, voff, soff, imm) asm volatile("buffer_load_dwordx4 %0, %1, %2, %3 offen offset:" #imm " sc1" : "=&v"(dst) : "v"(voff), "s"(rsv), "s"(soff) : "memory")
#define BLD2(dst, voff, soff, imm) asm volatile("buffer_load_dwordx2 %0, %1, %2, %3 offen offset:" #imm " sc1" : "=&v"(dst) : "v"(voff), "s"(rsv), "s"(soff) : "memory")
#define BLD1(dst, voff, soff, imm) asm volatile("buffer_load_dword %0, %1, %2, %3 offen offset:" #imm " sc1" : "=&v"(dst) : "v"(voff), "s"(rsv), "s"(soff) : "memory")
#define BLD4W(dst, voff, soff) asm volatile("buffer_load_dwordx4 %0, %1, %2, %3 offen sc1\n\ts_waitcnt vmcnt(0)" : "=&v"(dst) : "v"(voff), "s"(rsv), "s"(soff) : "memory")
#define BLD2W(dst, voff, soff) asm volatile("buffer_load_dwordx2 %0, %1, %2, %3 offen sc1\n\ts_waitcnt vmcnt(0)" : "=&v"(dst) : "v"(voff), "s"(rsv), "s"(soff) : "memory")
#define BLDOW(a0, a1, la, v0, v1, soff, soffl) asm volatile("buffer_load_dwordx4 %0, %3, %5, %6 offen sc1\n\tbuffer_load_dwordx4 %1, %3, %5, %6 offen offset:128 sc1\n\tbuffer_load_dword %2, %4, %5, %7 offen sc1\n\ts_waitcnt vmcnt(0)" \
    : "=&v"(a0), "=&v"(a1), "=&v"(la) : "v"(v0), "v"(v1), "s"(rsv), "s"(soff), "s"(soffl) : "memory")
#define BLDXW(a0, a1, v0, v1, soff) asm volatile("buffer_load_dwordx4 %0, %2, %4, %5 offen sc1\n\tbuffer_load_dwordx4 %1, %3, %4, %5 offen sc1\n\ts_waitcnt vmcnt(0)" \
    : "=&v"(a0), "=&v"(a1) : "v"(v0), "v"(v1), "s"(rsv), "s"(soff) : "memory")
#define XLO(a, c) (bflo(a) + (c))
#define XHI(a, c) (bfhi(a) + (c))
__device__ __forceinline__ float acc_lo(unsigned a, float c) { return __builtin_amdgcn_fdot2_f32_bf16(__builtin_bit_cast(bf16x2, a), __builtin_bit_cast(bf16x2, 0x00003F80u), c, false); }
__device__ __forceinline__ float acc_hi(unsigned a, float c) { return __builtin_amdgcn_fdot2_f32_bf16(__builtin_bit_cast(bf16x2, a), __builtin_bit_cast(bf16x2, 0x3F800000u), c, false); }

__device__ __forceinline__ v4u tr2(LAS unsigned char* p0, LAS unsigned char* p1) {
    typedef short v4i16_t __attribute__((ext_vector_type(4)));
    v4i16_t a = __builtin_amdgcn_ds_read_tr16_b64_v4i16((LAS v4i16_t*)p0);
    v4i16_t b = __builtin_amdgcn_ds_read_tr16_b64_v4i16((LAS v4i16_t*)p1);
    v2u x = __builtin_bit_cast(v2u, a), y = __builtin_bit_cast(v2u, b);
    return v4u{x.x, x.y, y.x, y.y};
}

#define STAMP(i) do {} while (0)

#define XT(i) do {} while (0)
#define TS(i) do {} while (0)
struct Res { v4u kf[8], vf[8]; float cs; };
template <int V> using IC = std::integral_constant<int, V>;

__global__ void __launch_bounds__(256, 1) mega(Params p) {
    unsigned long long entry_pc;
    asm volatile("s_getpc_b64 %0" : "=s"(entry_pc));
    extern __shared__ __attribute__((aligned(16))) unsigned char smem[];
    LAS unsigned char* L = (LAS unsigned char*)smem;
    const int c = blockIdx.x, tid = threadIdx.x, lane = tid & 63, w = __builtin_amdgcn_readfirstlane(tid >> 6);
    const int kvh = c & 7, s = (c >> 3) & 7, b = c >> 6, grp = b * 8 + kvh;
    const int g = lane >> 4, r16 = lane & 15, half = lane >> 5, i32 = lane & 31;
    const __amdgpu_buffer_rsrc_t rs = __builtin_amdgcn_make_buffer_rsrc(p.ws, 0, WS_END, 0x00020000);
#define ST4(v, off) __builtin_amdgcn_raw_buffer_store_b128((v), rs, (off), 0, AUX_SC1)
#define ST2(v, off) __builtin_amdgcn_raw_buffer_store_b64((v), rs, (off), 0, AUX_SC1)
#define ST1(v, off) __builtin_amdgcn_raw_buffer_store_b32((v), rs, (off), 0, AUX_SC1)
#define ST2S(v, voff, soff) __builtin_amdgcn_raw_buffer_store_b64((v), rs, (voff), (soff), AUX_SC1)
#define ST1S(v, voff, soff) __builtin_amdgcn_raw_buffer_store_b32((v), rs, (voff), (soff), AUX_SC1)
    KEEP_ACC();
    STAMP(0);
    unsigned tsv = 0; (void)tsv;
    unsigned tsx = 0; (void)tsx;
    XT(0);
    TS(0);
    const unsigned long long wsa = (unsigned long long)p.ws;
    const v4u rsv = {(unsigned)wsa, (unsigned)(wsa >> 32) & 0xFFFFu, (unsigned)WS_END, 0x00020000u};

    auto stage_w = [&](const u16* wmat, const unsigned ld, const unsigned coloff, const int u, const unsigned ldsb, const int h) {
        const unsigned ln = opq_v((unsigned)lane);
        const unsigned wrow = ln >> 3, wpos = ln & 7, wf = (wrow >> 1) & 1;
        const unsigned v0 = wrow * ld + ((((wpos >> 1) ^ wf) << 1) | (wpos & 1)) * 16;
        const unsigned v1 = wrow * ld + ((((wpos >> 1) ^ (wf | 2)) << 1) | (wpos & 1)) * 16;
        const unsigned char* wb = (const unsigned char*)(opq_s(wmat) + coloff) + (size_t)(u * 128 + h * 64) * ld;
        const int bb = opq_s(b);
#pragma unroll 1
        for (int i = 0; i < 8; i += 2) {
            const int pc = (i + 2 * bb) & 7;
            dma16(wb + (size_t)(pc * 8) * ld, v0, ldsb + h * 8192 + pc * 1024);
            dma16(wb + (size_t)(pc * 8 + 8) * ld, v1, ldsb + h * 8192 + pc * 1024 + 1024);
        }
    };
    const unsigned kvrow = (unsigned)((b * HKV + kvh) * SK + (SK - W) + s * 128);
    auto stage_k = [&](const u16* cache, const int u, const unsigned ldsb, const int h0, const int h1) {
        const unsigned ln = opq_v((unsigned)lane), gg = ln >> 4, rr = ln & 15;
        const unsigned char* sb = (const unsigned char*)opq_s(cache) + (size_t)(kvrow + u * 32) * 256;
        const unsigned v0 = gg * 256 + (rr ^ gg) * 16, v1 = gg * 256 + (rr ^ (4 + gg)) * 16, v2 = gg * 256 + (rr ^ (8 + gg)) * 16, v3 = gg * 256 + (rr ^ (12 + gg)) * 16;
        if (h0 == 0) dma16x4(sb, v0, v1, v2, v3, ldsb);
        if (h1 == 2) dma16x4(sb + 4096, v0, v1, v2, v3, ldsb + 4096);
    };
    auto stage_v = [&](const u16* cache, const int u, const unsigned ldsb) {
        const unsigned ln = opq_v((unsigned)lane), gg = ln >> 4, rr = ln & 15;
        const unsigned char* sb = (const unsigned char*)opq_s(cache) + (size_t)(kvrow + u * 32) * 256;
        const unsigned v0 = gg * 256 + ((((rr >> 1) ^ gg) << 1) | (rr & 1)) * 16, v1 = gg * 256 + ((((rr >> 1) ^ (4 + gg)) << 1) | (rr & 1)) * 16;
        dma16x4(sb, v0, v1, v0, v1, ldsb);
        dma16x4(sb + 4096, v0, v1, v0, v1, ldsb + 4096);
    };
    auto consume_w = [&](auto NC, const unsigned ldsb) {
        constexpr int N = decltype(NC)::value;
        const unsigned ln = opq_v((unsigned)lane), gg = ln >> 4, qq = (ln & 15) >> 2, pp = ln & 3;
        const unsigned f = (qq >> 1) | ((gg & 1) << 1);
        const unsigned base = ldsb + (8 * gg + qq) * 128 + (pp >> 1) * 16 + (pp & 1) * 8;
        tr_col<N>(base + (0 ^ f) * 32); tr_col<N + 16>(base + (1 ^ f) * 32); tr_col<N + 32>(base + (2 ^ f) * 32); tr_col<N + 48>(base + (3 ^ f) * 32);
        asm volatile("s_waitcnt lgkmcnt(0)" ::: "memory");
    };
#define LOAD_K(R, cache) do { \
        const unsigned ln_ = opq_v((unsigned)lane); \
        const unsigned char* kb_ = (const unsigned char*)opq_s(cache) + (size_t)(kvrow + opq_s(w) * 32) * 256; \
        const unsigned v_ = (ln_ & 15) * 256 + (ln_ >> 4) * 16, v2_ = v_ + 4096; \
        asm volatile("s_nop 4\n\tglobal_load_dwordx4 %0, %8, %10 nt\n\tglobal_load_dwordx4 %1, %8, %10 offset:64 nt\n\tglobal_load_dwordx4 %2, %8, %10 offset:128 nt\n\tglobal_load_dwordx4 %3, %8, %10 offset:192 nt\n\t" \
                     "global_load_dwordx4 %4, %9, %10 nt\n\tglobal_load_dwordx4 %5, %9, %10 offset:64 nt\n\tglobal_load_dwordx4 %6, %9, %10 offset:128 nt\n\tglobal_load_dwordx4 %7, %9, %10 offset:192 nt" \
                     : "=&v"(R.kf[0]), "=&v"(R.kf[1]), "=&v"(R.kf[2]), "=&v"(R.kf[3]), "=&v"(R.kf[4]), "=&v"(R.kf[5]), "=&v"(R.kf[6]), "=&v"(R.kf[7]) \
                     : "v"(v_), "v"(v2_), "s"(kb_) : "memory"); } while (0)
#define PIN_K(R) asm volatile("; COVERED_K" : "+v"(R.kf[0]), "+v"(R.kf[1]), "+v"(R.kf[2]), "+v"(R.kf[3]), "+v"(R.kf[4]), "+v"(R.kf[5]), "+v"(R.kf[6]), "+v"(R.kf[7]))
    auto consume_k = [&](v4u* dst, const unsigned ldsb) {
        const unsigned ln = opq_v((unsigned)lane), gg = ln >> 4, rr = ln & 15;
#pragma unroll
        for (int t = 0; t < 4; ++t) {
            LAS unsigned char* a = L + ldsb + rr * 256 + (((4 * t + gg) ^ rr) * 16);
            dst[t] = *(LAS v4u*)a; dst[4 + t] = *(LAS v4u*)(a + 4096);
        }
    };
    auto consume_v = [&](v4u* dst, const unsigned off) {
        const unsigned ln = opq_v((unsigned)lane), gg = ln >> 4, qq = (ln & 15) >> 2, pp = ln & 3;
        const unsigned vrow = 4 * gg + qq;
#pragma unroll
        for (int i = 0; i < 2; ++i) {
            LAS unsigned char* a = L + off + vrow * 256 + (((unsigned)(2 * w + i) ^ (vrow & 7)) * 32) + (pp >> 1) * 16 + (pp & 1) * 8;
#pragma unroll
            for (int t = 0; t < 4; ++t) dst[t * 2 + i] = tr2(a + t * RW, a + t * RW + 4096);
        }
    };
    const unsigned wq_col = (unsigned)(kvh * 512 + s * 64), wo_col = (unsigned)(kvh * 512 * 512 + s * 64);
    const unsigned myr = (unsigned)(w * RW);

    Res RA, RB;
#define RC(x)

    const unsigned xcol = (unsigned)(128 * w + 8 * r16 + 4 * (g >> 1) + 2 * (g & 1));
    unsigned xpk = 0;
    v2u gen = {0u, 0u};
    if (CODE_KB && w == 3) {
        const int piece = (c >> 3) % CODE_KB;
        dma16c((const unsigned char*)(entry_pc & ~255ull) + piece * 1024, (unsigned)lane * 16, L_TRASH);
    }
    if (w == 0) {
        dma16s(p.ws + WS_GEN, opq_v((unsigned)lane) * 16, L_GENB);
        dma16((const unsigned char*)p.x + b * 1024, opq_v((unsigned)lane) * 16, L_XS);
    }
    stage_w(p.wq0, 8192, wq_col, w, myr + U0, 0); stage_w(p.wq0, 8192, wq_col, w, myr + U0, 1);
    TS(15);
    LOAD_K(RA, p.kc0);
    stage_v(p.vc0, w, myr + U1 + 8192);
    TS(30);
    TS(45);
    ((LAS unsigned*)(L + L_ON))[lane] = 0x3F803F80u;

    const unsigned xg_v0 = (unsigned)(g * XB_K + w * 256 + r16 * 16), xg_v1 = xg_v0 + 4 * XB_K;
    const unsigned vq_st = ((lane & 1) == 0 && g == w) ? (unsigned)(lane * 2) : OOB;
    const unsigned vo_st = r16 < 4 ? (unsigned)(r16 * 256 + g * 8) : OOB;
    const unsigned vl_st = (lane < 4) ? (unsigned)(lane * 32) : OOB;
    const unsigned vx_st = (unsigned)((2 * w + half) * XB_R + i32 * 4);

    auto x_sum = [&](const v4u& a0, const v4u& a1, const bool active, float& o0, float& o1) -> bool {
        float v[8];
#pragma unroll
        for (int j = 0; j < 4; ++j) { v[2 * j] = XLO(a1[j], bflo(a0[j])); v[2 * j + 1] = XHI(a1[j], bfhi(a0[j])); }
        float u[4];
#pragma unroll
        for (int j = 0; j < 4; ++j) u[j] = swap32_add(v[j], v[j + 4]);
        o0 = swap16_add(u[0], u[2]); o1 = swap16_add(u[1], u[3]);
        const bool ok = !active || !__builtin_isunordered(o0, o1);
        if (__all(ok)) return true;
        const unsigned m = max(mx4(a0), mx4(a1));
        return __all(!active || m != EMPTY);
    };
    auto hello_ok = [&](const v2u& h) -> bool { return __all(h.x == gen.x && h.y == gen.y); };
    bool hello64 = false;

#pragma unroll 1
    for (int lp = 0; lp < NLAY; ++lp) {
        const int l = opq_s(lp < 0 ? 0 : lp);
        const bool live = lp >= 0;
        const bool TA = (lp & 1) == 0;
        const bool f0 = lp == 0, f1 = lp == 1;
        const int so_q = WS_QB + l * QB_L + grp * 1024, so_o = WS_OB + l * OB_L + grp * OB_G, so_l = WS_LB + l * LB_L + grp * 128, so_x = WS_XB + l * XB_L + b * XB_B;
        if (f0) {
            TS(60);
            VMWAIT(16);
            BAR();
            {
                const v2u g0 = *(LAS v2u*)(L + L_GENB + b * 256);
                const unsigned long long g1 = (((unsigned long long)g0.y << 32) | g0.x) + 1ull;
                gen = v2u{(unsigned)g1, (unsigned)(g1 >> 32)};
            }
            xpk = *(LAS unsigned*)(L + L_XS + xcol * 2);
        }
        STAMP(1 + l * 8 + 0);
        if (f0) XT(4);
        if (lp == 2) XT(28);
        KEEP_ACC();
        TS(1 + l * 15 + 0);
        if (lp > 0) *(LAS unsigned*)(L + L_XS + xcol * 2) = xpk;
        float rstd, tq;
        f32x4 acc[4];
        {
            LAS unsigned char* xb = L + (((f0 || f1) && r16 == 1) ? L_ON : L_XS + w * 256) + g * 16;
            v4u f[4];
#pragma unroll
            for (int t = 0; t < 4; ++t) f[t] = *(LAS v4u*)(xb + t * 64);
            SB();
            {
                const float x0 = bflo(xpk), x1 = bfhi(xpk);
                float p1 = x0 + x1, p2 = x0 * x0 + x1 * x1;
                dpp_acc<XOR1>(p1); dpp_acc<XOR1>(p2); dpp_acc<XOR2>(p1); dpp_acc<XOR2>(p2);
                dpp_acc<HMIR>(p1); dpp_acc<HMIR>(p2); dpp_acc<MIR>(p1); dpp_acc<MIR>(p2);
                *(LAS f32x2*)(L + L_ST + (w * 4 + g) * 8) = f32x2{p1, p2};
            }
            SB();
            if (f0) consume_w(IC<0>{}, myr + U0);
            TS(1 + l * 15 + 1);
            const unsigned a_st = (unsigned)(L_ST + r16 * 8);
            const int livei = opq_s(lp);
            if (TA) QBLOCK(0); else QBLOCK(128);
            LAS float* rq = (LAS float*)(L + L_RQ) + w * 64 + r16;
            rq[0] = acc[0][0]; rq[16] = acc[1][0]; rq[32] = acc[2][0]; rq[48] = acc[3][0];
            if (f0 || f1) {
                LAS float* rc = (LAS float*)(L + (g == 0 ? L_RC + w * 256 : L_TRASH + g * 256)) + r16;
                rc[0] = acc[0][1]; rc[16] = acc[1][1]; rc[32] = acc[2][1]; rc[48] = acc[3][1];
            }
        }
        asm volatile("s_waitcnt lgkmcnt(0)" ::: "memory");
        if (live) __builtin_amdgcn_s_barrier();
        asm volatile("" ::: "memory");
        TS(1 + l * 15 + 2);
        KEEP_ACC();
        v4u qa0 = {0u, 0u, 0u, 0u};
        {
            LAS float* rq = (LAS float*)(L + L_RQ) + lane;
            const float raw = (rq[0] + rq[64]) + (rq[128] + rq[192]);
            if (f0 || f1) {
                LAS float* rc = (LAS float*)(L + L_RC) + lane;
                const float cs = (rc[0] + rc[64]) + (rc[128] + rc[192]);
                if (f0) RA.cs = cs; else RB.cs = cs;
            }
            const float q = raw * rstd + tq * ((lp & 1) ? RB.cs : RA.cs);
            const unsigned qo = noempty(cvtpk(q, dpp<XOR1>(q)));
            if (live) {
                const int soff = opq_s(so_q + w * 256);
                const unsigned voff = (unsigned)(r16 * 16);
                if (f0) {
                    ST2S((v2u{qo, gen.x}), vq_st == OOB ? OOB : (unsigned)((lane >> 1) * 8), WS_QT + grp * 2048 + s * 256);
                    XT(5);
                    TS(1 + l * 15 + 3);
                    stage_w(p.wo0, 1024, wo_col, w, myr + U0, 0); stage_w(p.wo0, 1024, wo_col, w, myr + U0, 1);
                    {
                        const v4u e = {EMPTY, EMPTY, EMPTY, EMPTY};
                        const unsigned ln = opq_v((unsigned)lane);
#pragma unroll
                        for (int l4 = 0; l4 < NLAY; l4 += 4) {
                            ST4(e, WS_OB + (l4 + w) * OB_L + grp * OB_G + s * OB_P + ln * 16);
                            ST4(e, WS_XB + (l4 + w) * XB_L + b * XB_B + (ln >> 3) * XB_R + kvh * XB_K + s * 128 + (ln & 7) * 16);
                        }
                        if (w == 0) ST4(e, (ln >> 3) < NLAY ? (unsigned)(WS_QB + (ln >> 3) * QB_L + grp * 1024 + s * 128 + (ln & 7) * 16) : OOB);
                        if (w == 1) ST1(EMPTY, (ln >> 2) < NLAY ? (unsigned)(WS_LB + (ln >> 2) * LB_L + grp * 128 + (ln & 3) * 32 + s * 4) : OOB);
                    }
                    XT(6);
                    const int sofft = opq_s(WS_QT + grp * 2048 + w * 512);
                    const unsigned vofft = (unsigned)(i32 * 16);
                    v4u t;
                    BLD4(t, vofft, sofft, 0);
                    LOAD_K(RB, p.kc1);
                    stage_v(p.vc1, w, myr + U1);
                    asm volatile("s_waitcnt vmcnt(16) ; COVER" : "+v"(t) :: "memory");
                    PIN_K(RA);
                    if (UNLIKELY(!__all(t.y == gen.x && t.w == gen.x))) { for (unsigned sp = 0; sp < SPIN_CAP; ++sp) { BLD4W(t, vofft, sofft); if (__all(t.y == gen.x && t.w == gen.x)) break; } }
                    *(LAS v2u*)(L + L_QS + w * 256 + i32 * 8) = v2u{t.x, t.z};
                    XT(8);
                } else {
                    ST1S(qo, vq_st, so_q + s * 128);
                    TS(1 + l * 15 + 3);
                    delay<DLY_Q>();
                    BLD4W(qa0, voff, soff);
                }
                if (!f0 && UNLIKELY(!__all(mx4(qa0) != EMPTY))) {
                    for (unsigned sp = 0; sp < SPIN_CAP; ++sp) { RC(rcq); BLD4W(qa0, voff, soff); if (__all(mx4(qa0) != EMPTY)) break; }
                }
            }
        }
        TS(1 + l * 15 + 4);
        KEEP_ACC();
        if (!f0) *(LAS v4u*)(L + L_QS + w * 256 + r16 * 16) = qa0;
        STAMP(1 + l * 8 + 2);
        asm volatile("s_waitcnt lgkmcnt(0)" ::: "memory");
        if (live) __builtin_amdgcn_s_barrier();
        asm volatile("" ::: "memory");
        TS(1 + l * 15 + 5);
        if (f0 && tid == 0) ST2(gen, WS_HELLO + (b * 64 + kvh * 8 + s) * 8);
        f32x4 sc0, sc1;
        {
            LAS unsigned char* qb = L + L_QS + (lane & 3) * 256 + g * 16;
            v4u qf[4];
#pragma unroll
            for (int t = 0; t < 4; ++t) qf[t] = *(LAS v4u*)(qb + t * 64);
            SB();
            if (TA) QKBLOCK(RA); else QKBLOCK(RB);
            SB();
            if (f0) consume_v(RA.vf, U1 + 8192);
            asm volatile("s_nop 7" : "+v"(sc0), "+v"(sc1));
        }
        TS(1 + l * 15 + 6);
        KEEP_ACC();
        constexpr float CS = 0.08838834764831845f * 1.4426950408889634f;
        float mw = max3f(max3f(sc0[0], sc0[1], sc0[2]), max3f(sc0[3], sc1[0], sc1[1]), max2f(sc1[2], sc1[3]));
        mw = x16_maxf(mw) * CS;
        float pe[8];
#pragma unroll
        for (int j = 0; j < 4; ++j) { pe[j] = __builtin_amdgcn_exp2f(sc0[j] * CS - mw); pe[4 + j] = __builtin_amdgcn_exp2f(sc1[j] * CS - mw); }
        {
            const v4u pf = {cvtpk(pe[0], pe[1]), cvtpk(pe[2], pe[3]), cvtpk(pe[4], pe[5]), cvtpk(pe[6], pe[7])};
            *(LAS v4u*)(L + L_PS + w * 256 + (lane & 3) * 64 + g * 16) = pf;
            SB();
            float lw = ((pe[0] + pe[1]) + (pe[2] + pe[3])) + ((pe[4] + pe[5]) + (pe[6] + pe[7]));
            lw = x16_sum(lw);
            *(LAS f32x2*)(L + L_ML + (lane & 3) * 32 + w * 8) = f32x2{mw, lw};
        }
        TS(1 + l * 15 + 7);
        KEEP_ACC();
        asm volatile("s_waitcnt lgkmcnt(0)" ::: "memory");
        if (live) __builtin_amdgcn_s_barrier();
        asm volatile("" ::: "memory");
        v4u oa0 = {0u, 0u, 0u, 0u}, oa1 = oa0; unsigned la = 0;
        {
            LAS unsigned char* pb = L + L_PS + (lane & 3) * 64 + g * 16;
            const f32x4 ml0 = *(LAS f32x4*)(L + L_ML + (lane & 3) * 32), ml1 = *(LAS f32x4*)(L + L_ML + (lane & 3) * 32 + 16);
            v4u pf[4];
#pragma unroll
            for (int t = 0; t < 4; ++t) pf[t] = *(LAS v4u*)(pb + t * 256);
            SB();
            const float M = max2f(max3f(ml0[0], ml0[2], ml1[0]), ml1[2]);
            const float a0 = __builtin_amdgcn_exp2f(ml0[0] - M), a1 = __builtin_amdgcn_exp2f(ml0[2] - M);
            const float a2 = __builtin_amdgcn_exp2f(ml1[0] - M), a3 = __builtin_amdgcn_exp2f(ml1[2] - M);
            const float Ls = (a0 * ml0[1] + a1 * ml0[3]) + (a2 * ml1[1] + a3 * ml1[3]);
            const float inv = __builtin_amdgcn_rcpf(Ls);
            float lam = M + __builtin_amdgcn_logf(Ls);
            float b0 = a0 * inv, b1 = a1 * inv, b2 = a2 * inv, b3 = a3 * inv;
            PIN(b0); PIN(b1); PIN(b2); PIN(b3); PIN(lam);
            SB();
            f32x4 oa[4][2];
            if (TA) PVBLOCK(RA); else PVBLOCK(RB);
            TS(1 + l * 15 + 8);
            f32x4 o0 = oa[0][0] * b0, o1 = oa[0][1] * b0;
            o0 += oa[1][0] * b1; o1 += oa[1][1] * b1;
            o0 += oa[2][0] * b2; o1 += oa[2][1] * b2;
            o0 += oa[3][0] * b3; o1 += oa[3][1] * b3;
            const v2u w0 = {noempty(cvtpk(o0[0], o0[1])), noempty(cvtpk(o0[2], o0[3]))};
            const v2u w1 = {noempty(cvtpk(o1[0], o1[1])), noempty(cvtpk(o1[2], o1[3]))};
            if (live) {
                const int ob = so_o + s * OB_P;
                ST2S(w0, vo_st, ob + w * 64);
                ST2S(w1, vo_st, ob + w * 64 + 32);
                if (w == 0) ST1S(noempty(__float_as_uint(lam)), vl_st, so_l + s * 4);
                STAMP(1 + l * 8 + 3);
                TS(1 + l * 15 + 9);
                const int soff = opq_s(so_o + w * 256), soffl = opq_s(so_l + w * 32);
                const unsigned voff = (unsigned)((r16 & 7) * OB_P + (r16 >> 3) * 64 + g * 16);
                const unsigned voffl = (unsigned)((lane & 7) * 4);
                if (f0) {
                    v2u h;
                    const int hoff = opq_s(WS_HELLO + (b * 64) * 8);
                    const unsigned hv = (unsigned)(lane * 8);
                    const bool mine = (lane >> 3) == kvh;
                    BLD2(h, hv, hoff, 0);
                    consume_w(IC<64>{}, myr + U0);
                    stage_w(p.wq1, 8192, wq_col, w, myr + U0, 0); stage_w(p.wq1, 8192, wq_col, w, myr + U0, 1);
                    asm volatile("s_waitcnt vmcnt(16) ; COVER" : "+v"(h) :: "memory");
                    PIN_K(RB);
                    BAR();
                    consume_v(RB.vf, U1);
                    BAR();
                    if (UNLIKELY(!__all(!mine || (h.x == gen.x && h.y == gen.y)))) { for (unsigned sp = 0; sp < SPIN_CAP; ++sp) { BLD2W(h, hv, hoff); if (__all(!mine || (h.x == gen.x && h.y == gen.y))) break; } }
                    hello64 = hello_ok(h);
                    BLD4(oa0, voff, soff, 0); BLD4(oa1, voff, soff, 128); BLD1(la, voffl, soffl, 0);
                    stage_w(p.wo1, 1024, wo_col, w, myr + U1, 0); stage_w(p.wo1, 1024, wo_col, w, myr + U1, 1);
                    asm volatile("s_waitcnt vmcnt(16) ; COVER" : "+v"(oa0), "+v"(oa1), "+v"(la) :: "memory");
                } else if (f1) {
                    delay<DLY_O - 2>();
                    consume_w(IC<192>{}, myr + U1);
                    BLDOW(oa0, oa1, la, voff, voffl, soff, soffl);
                } else {
                    delay<DLY_O>();
                    BLDOW(oa0, oa1, la, voff, voffl, soff, soffl);
                }
                if (UNLIKELY(!__all(max(max(mx4(oa0), mx4(oa1)), la) != EMPTY))) {
                    for (unsigned sp = 0; sp < SPIN_CAP; ++sp) { RC(rco); BLDOW(oa0, oa1, la, voff, voffl, soff, soffl); if (__all(max(max(mx4(oa0), mx4(oa1)), la) != EMPTY)) break; }
                }
            }
        }
        STAMP(1 + l * 8 + 4);
        TS(1 + l * 15 + 10);
        KEEP_ACC();
        {
            f32x4 aL[4], aH[4];
            f32x4 wt; float tm, te;
            const unsigned a_wtw = (unsigned)(L_WT + w * 32 + (lane & 7) * 4), a_wtr = (unsigned)(L_WT + w * 32 + (g & 1) * 16);
            if (TA) OBLOCK(64); else OBLOCK(192);
            const f32x2 z2 = {0.f, 0.f};
            const f32x2 wL0 = half ? z2 : f32x2{wt[0], wt[1]}, wL1 = half ? z2 : f32x2{wt[2], wt[3]};
            const f32x2 wH0 = half ? f32x2{wt[0], wt[1]} : z2, wH1 = half ? f32x2{wt[2], wt[3]} : z2;
            TS(1 + l * 15 + 11);
            float v[4];
#pragma unroll
            for (int n = 0; n < 4; ++n) {
                f32x2 t = f32x2{aL[n][0], aL[n][1]} * wL0;
                t += f32x2{aL[n][2], aL[n][3]} * wL1; t += f32x2{aH[n][0], aH[n][1]} * wH0; t += f32x2{aH[n][2], aH[n][3]} * wH1;
                v[n] = t.x + t.y;
            }
            const float u0 = swap32_add(v[0], v[1]), u1 = swap32_add(v[2], v[3]);
            const float tot = swap16_add(u0, u1);
            *((LAS float*)(L + L_RX) + w * 64 + ((g & 1) * 2 + (g >> 1)) * 16 + r16) = tot;
        }
        asm volatile("s_waitcnt lgkmcnt(0)" ::: "memory");
        if (live) __builtin_amdgcn_s_barrier();
        asm volatile("" ::: "memory");
        TS(1 + l * 15 + 12);
        KEEP_ACC();
        {
            LAS unsigned char* rx = L + L_RX + i32 * 8;
            const f32x2 sum = (*(LAS f32x2*)(rx) + *(LAS f32x2*)(rx + 256)) + (*(LAS f32x2*)(rx + 512) + *(LAS f32x2*)(rx + 768));
            const unsigned ov = noempty(cvtpk(sum.x, sum.y));
            if (live) {
                ST1S(ov, vx_st, so_x + kvh * XB_K + s * 128);
                STAMP(1 + l * 8 + 5);
                TS(1 + l * 15 + 13);
                const bool fin = lp == NLAY - 1;
                const bool act = !fin || (s == 0 && w == (kvh >> 1) && (r16 >> 3) == (kvh & 1));
                if (!fin || (s == 0 && w == (kvh >> 1))) {
                    const int soff = opq_s(so_x + kvh * XB_R);
                    const unsigned v0 = act ? xg_v0 : OOB, v1 = act ? xg_v1 : OOB;
                    v4u a0, a1;
                    if (f0) {
                        XT(14);
                        if (UNLIKELY(!hello64)) {
                            v2u h;
                            const int hoff = opq_s(WS_HELLO + (b * 64) * 8);
                            const unsigned hv = (unsigned)(lane * 8);
                            for (unsigned sp = 0; sp < SPIN_CAP; ++sp) { BLD2W(h, hv, hoff); if (hello_ok(h)) break; }
                        }
                        if ((c & 63) == 0 && tid == 0) ST2(gen, WS_GEN + b * 256);
                        consume_w(IC<128>{}, myr + U0);
                        delay<DLY_X - 4>();
                        BLDXW(a0, a1, v0, v1, soff);
                        XT(17);
                    } else {
                        delay<DLY_X>();
                        BLDXW(a0, a1, v0, v1, soff);
                    }
                    float o0, o1;
                    if (UNLIKELY(!x_sum(a0, a1, act, o0, o1))) {
                        for (unsigned sp = 0; sp < SPIN_CAP; ++sp) { RC(rcx); BLDXW(a0, a1, v0, v1, soff); if (x_sum(a0, a1, act, o0, o1)) break; }
                    }
                    xpk = cvtpk(bflo(xpk) + o0, bfhi(xpk) + o1);
                    if (f0) XT(18);
                    if (f1) XT(27);
                    if (fin && act) *(GAS unsigned*)((GAS unsigned char*)p.out + (b * 512 + xcol) * 2) = xpk;
                }
            }
        }
    }
    STAMP(40);
}
}

extern "C" void kernel_launch(void* const* d_in, const int* in_sizes, int n_in, void* d_out, int out_size, void* d_ws, size_t ws_size, hipStream_t stream) {
    mk::Params p;
    p.x = (const u16*)d_in[0]; p.kc0 = (const u16*)d_in[1]; p.vc0 = (const u16*)d_in[2]; p.wq0 = (const u16*)d_in[3]; p.wo0 = (const u16*)d_in[4];
    p.kc1 = (const u16*)d_in[6]; p.vc1 = (const u16*)d_in[7]; p.wq1 = (const u16*)d_in[8]; p.wo1 = (const u16*)d_in[9];
    p.out = (u16*)d_out; p.ws = (unsigned char*)d_ws;
    (void)hipFuncSetAttribute((const void*)mk::mega, hipFuncAttributeMaxDynamicSharedMemorySize, mk::LDS_BYTES);
    hipLaunchKernelGGL(mk::mega, dim3(256), dim3(256), mk::LDS_BYTES, stream, p);
}
```

```cpp
#include <hip/hip_runtime.h>
#include <hip/hip_bf16.h>
#include <stdint.h>
#include <type_traits>

#define MK_STAMPS 0
#define NLAY 4
#define DRYRUN 0
#define PACE 63
#define CODE_KB 10
#define PRETOUCH 1
#define DLY_Q 6
#define DLY_O 6
#define DLY_X 14

#define GAS __attribute__((address_space(1)))
#define LAS __attribute__((address_space(3)))
typedef unsigned short u16;
typedef unsigned v4u __attribute__((ext_vector_type(4)));
typedef unsigned v2u __attribute__((ext_vector_type(2)));
typedef float f32x4 __attribute__((ext_vector_type(4)));
typedef float f32x2 __attribute__((ext_vector_type(2)));
typedef __bf16 bf16x8 __attribute__((ext_vector_type(8)));
typedef __bf16 bf16x2 __attribute__((ext_vector_type(2)));

namespace mk {
constexpr int HKV = 8, SK = 4096, DH = 128, W = 1024;
constexpr unsigned EMPTY = 0xFFFFFFFFu;
constexpr int AUX_SC1 = 16;
constexpr unsigned SPIN_CAP = 1u << 17;

constexpr int WS_GEN = 0;
constexpr int WS_HELLO = 1024;
constexpr int WS_QT = 4096;
constexpr int WS_QB = WS_QT + 32 * 2048;
constexpr int QB_L = 32 * 1024;
constexpr int WS_OB = WS_QB + NLAY * QB_L;
constexpr int OB_P = 1024, OB_G = 8 * OB_P, OB_L = 32 * OB_G;
constexpr int WS_LB = WS_OB + NLAY * OB_L;
constexpr int LB_L = 32 * 128;
constexpr int WS_XB = WS_LB + NLAY * LB_L;
constexpr int XB_K = 1024, XB_R = 8 * XB_K, XB_B = 8 * XB_R, XB_L = 4 * XB_B;
constexpr int WS_END = WS_XB + NLAY * XB_L;
constexpr int WS_STAMP = (WS_END + 4095) & ~4095;

constexpr int RW = 36864;
constexpr int U0 = 0, U1 = 16384;
constexpr int L_EX = 4 * RW;
constexpr int L_XS = L_EX;
constexpr int L_QS = L_XS + 1024;
constexpr int L_PS = L_QS + 1024;
constexpr int L_ON = L_PS + 1024;
constexpr int L_RQ = L_ON + 256;
constexpr int L_RC = L_RQ + 1024;
constexpr int L_RX = L_RC + 1024;
constexpr int L_ML = L_RX + 1024;
constexpr int L_ST = L_ML + 128;
constexpr int L_GEN = L_ST + 128;
constexpr int L_WT = L_GEN + 16;
constexpr int L_TRASH = L_WT + 128;
constexpr int L_GENB = L_TRASH + 1024;
constexpr int L_END = L_GENB + 1024;
constexpr int LDS_BYTES = 160 * 1024;
static_assert(L_END <= LDS_BYTES, "lds");
static_assert((L_WT % 16) == 0 && (L_ST % 16) == 0 && (L_RQ % 16) == 0 && (L_TRASH % 16) == 0, "align");

struct Params {
    const u16* x; const u16* kc0; const u16* vc0; const u16* wq0; const u16* wo0;
    const u16* kc1; const u16* vc1; const u16* wq1; const u16* wo1;
    u16* out; unsigned char* ws;
};

__device__ __forceinline__ f32x4 mfma(v4u a, v4u b, f32x4 c) {
    return __builtin_amdgcn_mfma_f32_16x16x32_bf16(__builtin_bit_cast(bf16x8, a), __builtin_bit_cast(bf16x8, b), c, 0, 0, 0);
}
__device__ __forceinline__ unsigned cvtpk(float lo, float hi) {
    bf16x2 r = {(__bf16)lo, (__bf16)hi};
    return __builtin_bit_cast(unsigned, r);
}
__device__ __forceinline__ float bflo(unsigned u) { return __builtin_bit_cast(float, u << 16); }
__device__ __forceinline__ float bfhi(unsigned u) { return __builtin_bit_cast(float, u & 0xFFFF0000u); }
template <int CTRL> __device__ __forceinline__ float dpp(float x) {
    return __builtin_bit_cast(float, __builtin_amdgcn_mov_dpp(__builtin_bit_cast(int, x), CTRL, 0xf, 0xf, true));
}
constexpr int XOR1 = 0xB1, XOR2 = 0x4E, HMIR = 0x141, MIR = 0x140, SHL1 = 0x101;
__device__ __forceinline__ float x16_max(float x) {
    auto s = __builtin_amdgcn_permlane16_swap(__float_as_uint(x), __float_as_uint(x), false, false);
    x = fmaxf(__uint_as_float(s[0]), __uint_as_float(s[1]));
    auto t = __builtin_amdgcn_permlane32_swap(__float_as_uint(x), __float_as_uint(x), false, false);
    return fmaxf(__uint_as_float(t[0]), __uint_as_float(t[1]));
}
__device__ __forceinline__ float x16_sum(float x) {
    auto s = __builtin_amdgcn_permlane16_swap(__float_as_uint(x), __float_as_uint(x), false, false);
    x = __uint_as_float(s[0]) + __uint_as_float(s[1]);
    auto t = __builtin_amdgcn_permlane32_swap(__float_as_uint(x), __float_as_uint(x), false, false);
    return __uint_as_float(t[0]) + __uint_as_float(t[1]);
}
__device__ __forceinline__ float row16_sum(float x) { x += dpp<XOR1>(x); x += dpp<XOR2>(x); x += dpp<HMIR>(x); x += dpp<MIR>(x); return x; }
__device__ __forceinline__ float row8_sum(float x) { x += dpp<XOR1>(x); x += dpp<XOR2>(x); x += dpp<HMIR>(x); return x; }
__device__ __forceinline__ float row8_max(float x) { x = fmaxf(x, dpp<XOR1>(x)); x = fmaxf(x, dpp<XOR2>(x)); x = fmaxf(x, dpp<HMIR>(x)); return x; }
__device__ __forceinline__ float swap32_add(float a, float b) {
    auto s = __builtin_amdgcn_permlane32_swap(__float_as_uint(a), __float_as_uint(b), false, false);
    return __uint_as_float(s[0]) + __uint_as_float(s[1]);
}
__device__ __forceinline__ float swap16_add(float a, float b) {
    auto s = __builtin_amdgcn_permlane16_swap(__float_as_uint(a), __float_as_uint(b), false, false);
    return __uint_as_float(s[0]) + __uint_as_float(s[1]);
}
__device__ __forceinline__ unsigned mx4(v4u v) { return max(max(v.x, v.y), max(v.z, v.w)); }
__device__ __forceinline__ unsigned noempty(unsigned u) { return min(u, 0xFFFFFFFEu); }
__device__ __forceinline__ f32x2 lo2(v4u v) { return f32x2{__uint_as_float(v.x), __uint_as_float(v.y)}; }
__device__ __forceinline__ f32x2 hi2(v4u v) { return f32x2{__uint_as_float(v.z), __uint_as_float(v.w)}; }

#define SB() __builtin_amdgcn_sched_barrier(0)
#define KEEP_ACC() asm volatile("" ::: "a0", "a1", "a2", "a3", "a4", "a5", "a6", "a7", "a8", "a9", "a10", "a11", "a12", "a13", "a14", "a15", "a16", "a17", "a18", "a19", "a20", "a21", "a22", "a23", "a24", "a25", "a26", "a27", "a28", "a29", "a30", "a31", "a32", "a33", "a34", "a35", "a36", "a37", "a38", "a39", "a40", "a41", "a42", "a43", "a44", "a45", "a46", "a47", "a48", "a49", "a50", "a51", "a52", "a53", "a54", "a55", "a56", "a57", "a58", "a59", "a60", "a61", "a62", "a63", "a64", "a65", "a66", "a67", "a68", "a69", "a70", "a71", "a72", "a73", "a74", "a75", "a76", "a77", "a78", "a79", "a80", "a81", "a82", "a83", "a84", "a85", "a86", "a87", "a88", "a89", "a90", "a91", "a92", "a93", "a94", "a95", "a96", "a97", "a98", "a99", "a100", "a101", "a102", "a103", "a104", "a105", "a106", "a107", "a108", "a109", "a110", "a111", "a112", "a113", "a114", "a115", "a116", "a117", "a118", "a119", "a120", "a121", "a122", "a123", "a124", "a125", "a126", "a127", "a128", "a129", "a130", "a131", "a132", "a133", "a134", "a135", "a136", "a137", "a138", "a139", "a140", "a141", "a142", "a143", "a144", "a145", "a146", "a147", "a148", "a149", "a150", "a151", "a152", "a153", "a154", "a155", "a156", "a157", "a158", "a159", "a160", "a161", "a162", "a163", "a164", "a165", "a166", "a167", "a168", "a169", "a170", "a171", "a172", "a173", "a174", "a175", "a176", "a177", "a178", "a179", "a180", "a181", "a182", "a183", "a184", "a185", "a186", "a187", "a188", "a189", "a190", "a191", "a192", "a193", "a194", "a195", "a196", "a197", "a198", "a199", "a200", "a201", "a202", "a203", "a204", "a205", "a206", "a207", "a208", "a209", "a210", "a211", "a212", "a213", "a214", "a215", "a216", "a217", "a218", "a219", "a220", "a221", "a222", "a223", "a224", "a225", "a226", "a227", "a228", "a229", "a230", "a231", "a232", "a233", "a234", "a235", "a236", "a237", "a238", "a239", "a240", "a241", "a242", "a243", "a244", "a245", "a246", "a247", "a248", "a249", "a250", "a251", "a252", "a253", "a254", "a255")
#define MFMA0(acc, A, B, CA) asm volatile("v_mfma_f32_16x16x32_bf16 %0, %1, %2, 0" : "=&v"(acc) : CA(A), "v"(B))
#define MFMA(acc, A, B, CA) asm volatile("v_mfma_f32_16x16x32_bf16 %0, %1, %2, %0" : "+v"(acc) : CA(A), "v"(B))
#define PIN(x) asm volatile("" : "+v"(x))
#define MFMAW0(acc, A, N) asm volatile("v_mfma_f32_16x16x32_bf16 %0, %1, a[%c2:%c3], 0" : "=&v"(acc) : "v"(A), "i"(N), "i"((N) + 3))
#define MFMAW(acc, A, N) asm volatile("v_mfma_f32_16x16x32_bf16 %0, %1, a[%c2:%c3], %0" : "+v"(acc) : "v"(A), "i"(N), "i"((N) + 3))
template <int N> __device__ __forceinline__ void tr_col(unsigned addr) {
    asm volatile("ds_read_b64_tr_b16 a[%c1:%c2], %0\n\tds_read_b64_tr_b16 a[%c3:%c4], %0 offset:512\n\t"
                 "ds_read_b64_tr_b16 a[%c5:%c6], %0 offset:4096\n\tds_read_b64_tr_b16 a[%c7:%c8], %0 offset:4608"
                 :: "v"(addr), "i"(N), "i"(N + 1), "i"(N + 2), "i"(N + 3), "i"(N + 4), "i"(N + 5), "i"(N + 6), "i"(N + 7) : "memory");
    asm volatile("ds_read_b64_tr_b16 a[%c1:%c2], %0 offset:8192\n\tds_read_b64_tr_b16 a[%c3:%c4], %0 offset:8704\n\t"
                 "ds_read_b64_tr_b16 a[%c5:%c6], %0 offset:12288\n\tds_read_b64_tr_b16 a[%c7:%c8], %0 offset:12800"
                 :: "v"(addr), "i"(N + 8), "i"(N + 9), "i"(N + 10), "i"(N + 11), "i"(N + 12), "i"(N + 13), "i"(N + 14), "i"(N + 15) : "memory");
}
#define QBLOCK(NB) asm volatile("v_mfma_f32_16x16x32_bf16 %[c0], %[f0], a[%c[N]+0:%c[N]+3], 0\n\t" \
        "v_mfma_f32_16x16x32_bf16 %[c1], %[f0], a[%c[N]+16:%c[N]+19], 0\n\t" \
        "s_waitcnt lgkmcnt(0)\n\t" \
        "s_cmp_ge_i32 %[live], 0\n\t" \
        "s_cbranch_scc0 1f\n\t" \
        "s_barrier\n\t" \
        "1:\n\t" \
        "ds_read_b32 %[p1], %[ast]\n\t" \
        "ds_read_b32 %[p2], %[ast] offset:4\n\t" \
        "v_mfma_f32_16x16x32_bf16 %[c2], %[f0], a[%c[N]+32:%c[N]+35], 0\n\t" \
        "v_mfma_f32_16x16x32_bf16 %[c3], %[f0], a[%c[N]+48:%c[N]+51], 0\n\t" \
        "v_mfma_f32_16x16x32_bf16 %[c0], %[f1], a[%c[N]+4:%c[N]+7], %[c0]\n\t" \
        "v_mfma_f32_16x16x32_bf16 %[c1], %[f1], a[%c[N]+20:%c[N]+23], %[c1]\n\t" \
        "s_waitcnt lgkmcnt(0)\n\t" \
        "v_add_f32_dpp %[p1], %[p1], %[p1] quad_perm:[1,0,3,2] row_mask:0xf bank_mask:0xf\n\t" \
        "v_add_f32_dpp %[p2], %[p2], %[p2] quad_perm:[1,0,3,2] row_mask:0xf bank_mask:0xf\n\t" \
        "v_mfma_f32_16x16x32_bf16 %[c2], %[f1], a[%c[N]+36:%c[N]+39], %[c2]\n\t" \
        "v_add_f32_dpp %[p1], %[p1], %[p1] quad_perm:[2,3,0,1] row_mask:0xf bank_mask:0xf\n\t" \
        "v_add_f32_dpp %[p2], %[p2], %[p2] quad_perm:[2,3,0,1] row_mask:0xf bank_mask:0xf\n\t" \
        "v_mfma_f32_16x16x32_bf16 %[c3], %[f1], a[%c[N]+52:%c[N]+55], %[c3]\n\t" \
        "v_add_f32_dpp %[p1], %[p1], %[p1] row_half_mirror row_mask:0xf bank_mask:0xf\n\t" \
        "v_add_f32_dpp %[p2], %[p2], %[p2] row_half_mirror row_mask:0xf bank_mask:0xf\n\t" \
        "v_mfma_f32_16x16x32_bf16 %[c0], %[f2], a[%c[N]+8:%c[N]+11], %[c0]\n\t" \
        "v_add_f32_dpp %[p1], %[p1], %[p1] row_mirror row_mask:0xf bank_mask:0xf\n\t" \
        "v_add_f32_dpp %[p2], %[p2], %[p2] row_mirror row_mask:0xf bank_mask:0xf\n\t" \
        "v_mfma_f32_16x16x32_bf16 %[c1], %[f2], a[%c[N]+24:%c[N]+27], %[c1]\n\t" \
        "v_mul_f32 %[p1], 0x3b000000, %[p1]\n\t" \
        "v_mul_f32 %[p2], 0x3b000000, %[p2]\n\t" \
        "v_mfma_f32_16x16x32_bf16 %[c2], %[f2], a[%c[N]+40:%c[N]+43], %[c2]\n\t" \
        "v_fma_f32 %[p2], -%[p1], %[p1], %[p2]\n\t" \
        "v_max_f32 %[p2], 0, %[p2]\n\t" \
        "v_mfma_f32_16x16x32_bf16 %[c3], %[f2], a[%c[N]+56:%c[N]+59], %[c3]\n\t" \
        "v_add_f32 %[p2], 0x3727c5ac, %[p2]\n\t" \
        "v_rsq_f32 %[p2], %[p2]\n\t" \
        "v_mfma_f32_16x16x32_bf16 %[c0], %[f3], a[%c[N]+12:%c[N]+15], %[c0]\n\t" \
        "v_mfma_f32_16x16x32_bf16 %[c1], %[f3], a[%c[N]+28:%c[N]+31], %[c1]\n\t" \
        "v_mul_f32 %[p1], %[p1], %[p2]\n\t" \
        "v_sub_f32 %[p1], 0, %[p1]\n\t" \
        "v_mfma_f32_16x16x32_bf16 %[c2], %[f3], a[%c[N]+44:%c[N]+47], %[c2]\n\t" \
        "v_mfma_f32_16x16x32_bf16 %[c3], %[f3], a[%c[N]+60:%c[N]+63], %[c3]\n\t" \
        "s_nop 7\n\t" "" \
        : [c0] "=&v"(acc[0]), [c1] "=&v"(acc[1]), [c2] "=&v"(acc[2]), [c3] "=&v"(acc[3]), [p1] "=&v"(tq), [p2] "=&v"(rstd) \
        : [f0] "v"(f[0]), [f1] "v"(f[1]), [f2] "v"(f[2]), [f3] "v"(f[3]), [ast] "v"(a_st), [live] "s"(livei), [N] "i"(NB) : "memory", "scc")
#define QKBLOCK(R) asm volatile("v_mfma_f32_16x16x32_bf16 %[s0], %[k0], %[q0], 0\n\t" \
        "v_mfma_f32_16x16x32_bf16 %[s1], %[k4], %[q0], 0\n\t" \
        "v_mfma_f32_16x16x32_bf16 %[s0], %[k1], %[q1], %[s0]\n\t" \
        "v_mfma_f32_16x16x32_bf16 %[s1], %[k5], %[q1], %[s1]\n\t" \
        "v_mfma_f32_16x16x32_bf16 %[s0], %[k2], %[q2], %[s0]\n\t" \
        "v_mfma_f32_16x16x32_bf16 %[s1], %[k6], %[q2], %[s1]\n\t" \
        "v_mfma_f32_16x16x32_bf16 %[s0], %[k3], %[q3], %[s0]\n\t" \
        "v_mfma_f32_16x16x32_bf16 %[s1], %[k7], %[q3], %[s1]\n\t" "" \
        : [s0] "=&v"(sc0), [s1] "=&v"(sc1) \
        : [k0] "v"(R.kf[0]), [k1] "v"(R.kf[1]), [k2] "v"(R.kf[2]), [k3] "v"(R.kf[3]), [k4] "v"(R.kf[4]), [k5] "v"(R.kf[5]), [k6] "v"(R.kf[6]), [k7] "v"(R.kf[7]), [q0] "v"(qf[0]), [q1] "v"(qf[1]), [q2] "v"(qf[2]), [q3] "v"(qf[3]))
#define PVBLOCK(R) asm volatile("v_mfma_f32_16x16x32_bf16 %[o0], %[v0], %[p0], 0\n\t" \
        "v_mfma_f32_16x16x32_bf16 %[o1], %[v1], %[p0], 0\n\t" \
        "v_mfma_f32_16x16x32_bf16 %[o2], %[v2], %[p1], 0\n\t" \
        "v_mfma_f32_16x16x32_bf16 %[o3], %[v3], %[p1], 0\n\t" \
        "v_mfma_f32_16x16x32_bf16 %[o4], %[v4], %[p2], 0\n\t" \
        "v_mfma_f32_16x16x32_bf16 %[o5], %[v5], %[p2], 0\n\t" \
        "v_mfma_f32_16x16x32_bf16 %[o6], %[v6], %[p3], 0\n\t" \
        "v_mfma_f32_16x16x32_bf16 %[o7], %[v7], %[p3], 0\n\t" \
        "s_nop 7\n\t" "" \
        : [o0] "=&v"(oa[0][0]), [o1] "=&v"(oa[0][1]), [o2] "=&v"(oa[1][0]), [o3] "=&v"(oa[1][1]), [o4] "=&v"(oa[2][0]), [o5] "=&v"(oa[2][1]), [o6] "=&v"(oa[3][0]), [o7] "=&v"(oa[3][1]) \
        : [v0] "v"(R.vf[0]), [v1] "v"(R.vf[1]), [v2] "v"(R.vf[2]), [v3] "v"(R.vf[3]), [v4] "v"(R.vf[4]), [v5] "v"(R.vf[5]), [v6] "v"(R.vf[6]), [v7] "v"(R.vf[7]), [p0] "v"(pf[0]), [p1] "v"(pf[1]), [p2] "v"(pf[2]), [p3] "v"(pf[3]))
#define OBLOCK(NB) asm volatile("v_mfma_f32_16x16x32_bf16 %[L0], %[a0], a[%c[N]+0:%c[N]+3], 0\n\t" \
        "v_max_f32_dpp %[m], %[la], %[la] quad_perm:[1,0,3,2] row_mask:0xf bank_mask:0xf\n\t" \
        "v_mfma_f32_16x16x32_bf16 %[H0], %[a0], a[%c[N]+4:%c[N]+7], 0\n\t" \
        "s_nop 0\n\t" \
        "v_max_f32_dpp %[m], %[m], %[m] quad_perm:[2,3,0,1] row_mask:0xf bank_mask:0xf\n\t" \
        "v_mfma_f32_16x16x32_bf16 %[L1], %[a0], a[%c[N]+16:%c[N]+19], 0\n\t" \
        "s_nop 0\n\t" \
        "v_max_f32_dpp %[m], %[m], %[m] row_half_mirror row_mask:0xf bank_mask:0xf\n\t" \
        "v_mfma_f32_16x16x32_bf16 %[H1], %[a0], a[%c[N]+20:%c[N]+23], 0\n\t" \
        "v_sub_f32 %[m], %[la], %[m]\n\t" \
        "v_exp_f32 %[e], %[m]\n\t" \
        "v_mfma_f32_16x16x32_bf16 %[L2], %[a0], a[%c[N]+32:%c[N]+35], 0\n\t" \
        "s_nop 0\n\t" \
        "v_add_f32_dpp %[m], %[e], %[e] quad_perm:[1,0,3,2] row_mask:0xf bank_mask:0xf\n\t" \
        "v_mfma_f32_16x16x32_bf16 %[H2], %[a0], a[%c[N]+36:%c[N]+39], 0\n\t" \
        "s_nop 0\n\t" \
        "v_add_f32_dpp %[m], %[m], %[m] quad_perm:[2,3,0,1] row_mask:0xf bank_mask:0xf\n\t" \
        "v_mfma_f32_16x16x32_bf16 %[L3], %[a0], a[%c[N]+48:%c[N]+51], 0\n\t" \
        "s_nop 0\n\t" \
        "v_add_f32_dpp %[m], %[m], %[m] row_half_mirror row_mask:0xf bank_mask:0xf\n\t" \
        "v_mfma_f32_16x16x32_bf16 %[H3], %[a0], a[%c[N]+52:%c[N]+55], 0\n\t" \
        "v_rcp_f32 %[m], %[m]\n\t" \
        "v_mfma_f32_16x16x32_bf16 %[L0], %[a1], a[%c[N]+8:%c[N]+11], %[L0]\n\t" \
        "v_mul_f32 %[e], %[e], %[m]\n\t" \
        "ds_write_b32 %[aw], %[e]\n\t" \
        "v_mfma_f32_16x16x32_bf16 %[H0], %[a1], a[%c[N]+12:%c[N]+15], %[H0]\n\t" \
        "ds_read_b128 %[wt], %[ar]\n\t" \
        "v_mfma_f32_16x16x32_bf16 %[L1], %[a1], a[%c[N]+24:%c[N]+27], %[L1]\n\t" \
        "v_mfma_f32_16x16x32_bf16 %[H1], %[a1], a[%c[N]+28:%c[N]+31], %[H1]\n\t" \
        "v_mfma_f32_16x16x32_bf16 %[L2], %[a1], a[%c[N]+40:%c[N]+43], %[L2]\n\t" \
        "v_mfma_f32_16x16x32_bf16 %[H2], %[a1], a[%c[N]+44:%c[N]+47], %[H2]\n\t" \
        "v_mfma_f32_16x16x32_bf16 %[L3], %[a1], a[%c[N]+56:%c[N]+59], %[L3]\n\t" \
        "v_mfma_f32_16x16x32_bf16 %[H3], %[a1], a[%c[N]+60:%c[N]+63], %[H3]\n\t" \
        "s_waitcnt lgkmcnt(0)\n\t" \
        "s_nop 6\n\t" "" \
        : [L0] "=&v"(aL[0]), [H0] "=&v"(aH[0]), [L1] "=&v"(aL[1]), [H1] "=&v"(aH[1]), [L2] "=&v"(aL[2]), [H2] "=&v"(aH[2]), [L3] "=&v"(aL[3]), [H3] "=&v"(aH[3]), \
          [wt] "=&v"(wt), [m] "=&v"(tm), [e] "=&v"(te) \
        : [a0] "v"(oa0), [a1] "v"(oa1), [la] "v"(la), [aw] "v"(a_wtw), [ar] "v"(a_wtr), [N] "i"(NB) : "memory")
__device__ __forceinline__ float max3f(float a, float b, float c) { float r; asm("v_max3_f32 %0, %1, %2, %3" : "=v"(r) : "v"(a), "v"(b), "v"(c)); return r; }
__device__ __forceinline__ float max2f(float a, float b) { float r; asm("v_max_f32 %0, %1, %2" : "=v"(r) : "v"(a), "v"(b)); return r; }
__device__ __forceinline__ float x16_maxf(float x) {
    auto s = __builtin_amdgcn_permlane16_swap(__float_as_uint(x), __float_as_uint(x), false, false);
    x = max2f(__uint_as_float(s[0]), __uint_as_float(s[1]));
    auto t = __builtin_amdgcn_permlane32_swap(__float_as_uint(x), __float_as_uint(x), false, false);
    return max2f(__uint_as_float(t[0]), __uint_as_float(t[1]));
}
constexpr unsigned OOB = 0x80000000u;
template <int CTRL> __device__ __forceinline__ void dpp_acc(float& x) { x += dpp<CTRL>(x); asm("" : "+v"(x)); }
template <int CTRL> __device__ __forceinline__ void dpp_mx(float& x) { x = max2f(x, dpp<CTRL>(x)); }
#define UNLIKELY(x) __builtin_expect(!!(x), 0)
#define BAR() do { asm volatile("s_waitcnt lgkmcnt(0)" ::: "memory"); __builtin_amdgcn_s_barrier(); asm volatile("" ::: "memory"); } while (0)
#define VMWAIT(n) asm volatile("s_waitcnt vmcnt(" #n ")" ::: "memory")
template <int N> __device__ __forceinline__ void delay() {
#pragma unroll
    for (int i = 0; i < N; ++i) asm volatile("s_nop 15");
}

__device__ __forceinline__ void dma16(const unsigned char* sbase, unsigned voff, unsigned lds) {
    unsigned keep;
    asm volatile("s_mov_b32 %0, m0\n\ts_mov_b32 m0, %3\n\ts_nop 2\n\tglobal_load_lds_dwordx4 %1, %2\n\ts_mov_b32 m0, %0\n\ts_waitcnt vmcnt(%c4)"
                 : "=&s"(keep) : "v"(voff), "s"(sbase), "s"(lds), "i"(PACE - 1) : "memory");
}
__device__ __forceinline__ void dma16c(const unsigned char* sbase, unsigned voff, unsigned lds) {
    unsigned keep;
    asm volatile("s_mov_b32 %0, m0\n\ts_mov_b32 m0, %3\n\ts_nop 2\n\tglobal_load_lds_dwordx4 %1, %2\n\ts_mov_b32 m0, %0"
                 : "=&s"(keep) : "v"(voff), "s"(sbase), "s"(lds) : "memory");
}
__device__ __forceinline__ void dma16s(const unsigned char* sbase, unsigned voff, unsigned lds) {
    unsigned keep;
    asm volatile("s_mov_b32 %0, m0\n\ts_mov_b32 m0, %3\n\ts_nop 2\n\tglobal_load_lds_dwordx4 %1, %2 sc1\n\ts_mov_b32 m0, %0"
                 : "=&s"(keep) : "v"(voff), "s"(sbase), "s"(lds) : "memory");
}
__device__ __forceinline__ void dma16x4(const unsigned char* sbase, unsigned v0, unsigned v1, unsigned v2, unsigned v3, unsigned lds) {
    unsigned keep;
    asm volatile("s_mov_b32 %0, m0\n\ts_mov_b32 m0, %6\n\ts_nop 2\n\t"
                 "global_load_lds_dwordx4 %1, %5\n\ts_waitcnt vmcnt(%c7)\n\tglobal_load_lds_dwordx4 %2, %5 offset:1024\n\ts_waitcnt vmcnt(%c7)\n\t"
                 "global_load_lds_dwordx4 %3, %5 offset:2048\n\ts_waitcnt vmcnt(%c7)\n\tglobal_load_lds_dwordx4 %4, %5 offset:3072\n\ts_waitcnt vmcnt(%c7)\n\ts_mov_b32 m0, %0"
                 : "=&s"(keep) : "v"(v0), "v"(v1), "v"(v2), "v"(v3), "s"(sbase), "s"(lds), "i"(PACE - 1) : "memory");
}
template <class T> __device__ __forceinline__ T opq_s(T v) { asm volatile("" : "+s"(v)); return v; }
template <class T> __device__ __forceinline__ T opq_v(T v) { asm volatile("" : "+v"(v)); return v; }
#define BLD4(dst, voff, soff, imm) asm volatile("buffer_load_dwordx4 %0, %1, %2, %3 offen offset:" #imm " sc1" : "=&v"(dst) : "v"(voff), "s"(rsv), "s"(soff) : "memory")
#define BLD2(dst, voff, soff, imm) asm volatile("buffer_load_dwordx2 %0, %1, %2, %3 offen offset:" #imm " sc1" : "=&v"(dst) : "v"(voff), "s"(rsv), "s"(soff) : "memory")
#define BLD1(dst, voff, soff, imm) asm volatile("buffer_load_dword %0, %1, %2, %3 offen offset:" #imm " sc1" : "=&v"(dst) : "v"(voff), "s"(rsv), "s"(soff) : "memory")
#define BLD4W(dst, voff, soff) asm volatile("buffer_load_dwordx4 %0, %1, %2, %3 offen sc1\n\ts_waitcnt vmcnt(0)" : "=&v"(dst) : "v"(voff), "s"(rsv), "s"(soff) : "memory")
#define BLD2W(dst, voff, soff) asm volatile("buffer_load_dwordx2 %0, %1, %2, %3 offen sc1\n\ts_waitcnt vmcnt(0)" : "=&v"(dst) : "v"(voff), "s"(rsv), "s"(soff) : "memory")
#define BLDOW(a0, a1, la, v0, v1, soff, soffl) asm volatile("buffer_load_dwordx4 %0, %3, %5, %6 offen sc1\n\tbuffer_load_dwordx4 %1, %3, %5, %6 offen offset:128 sc1\n\tbuffer_load_dword %2, %4, %5, %7 offen sc1\n\ts_waitcnt vmcnt(0)" \
    : "=&v"(a0), "=&v"(a1), "=&v"(la) : "v"(v0), "v"(v1), "s"(rsv), "s"(soff), "s"(soffl) : "memory")
#define BLDXW(a0, a1, v0, v1, soff) asm volatile("buffer_load_dwordx4 %0, %2, %4, %5 offen sc1\n\tbuffer_load_dwordx4 %1, %3, %4, %5 offen sc1\n\ts_waitcnt vmcnt(0)" \
    : "=&v"(a0), "=&v"(a1) : "v"(v0), "v"(v1), "s"(rsv), "s"(soff) : "memory")
#define XLO(a, c) (bflo(a) + (c))
#define XHI(a, c) (bfhi(a) + (c))
__device__ __forceinline__ float acc_lo(unsigned a, float c) { return __builtin_amdgcn_fdot2_f32_bf16(__builtin_bit_cast(bf16x2, a), __builtin_bit_cast(bf16x2, 0x00003F80u), c, false); }
__device__ __forceinline__ float acc_hi(unsigned a, float c) { return __builtin_amdgcn_fdot2_f32_bf16(__builtin_bit_cast(bf16x2, a), __builtin_bit_cast(bf16x2, 0x3F800000u), c, false); }

__device__ __forceinline__ v4u tr2(LAS unsigned char* p0, LAS unsigned char* p1) {
    typedef short v4i16_t __attribute__((ext_vector_type(4)));
    v4i16_t a = __builtin_amdgcn_ds_read_tr16_b64_v4i16((LAS v4i16_t*)p0);
    v4i16_t b = __builtin_amdgcn_ds_read_tr16_b64_v4i16((LAS v4i16_t*)p1);
    v2u x = __builtin_bit_cast(v2u, a), y = __builtin_bit_cast(v2u, b);
    return v4u{x.x, x.y, y.x, y.y};
}

#define STAMP(i) do {} while (0)

#define XT(i) do {} while (0)
#define TS(i) do {} while (0)
struct Res { v4u kf[8], vf[8]; float cs; };
template <int V> using IC = std::integral_constant<int, V>;

__global__ void __launch_bounds__(256, 1) mega(Params p) {
    unsigned long long entry_pc;
    asm volatile("s_getpc_b64 %0" : "=s"(entry_pc));
    extern __shared__ __attribute__((aligned(16))) unsigned char smem[];
    LAS unsigned char* L = (LAS unsigned char*)smem;
    const int c = blockIdx.x, tid = threadIdx.x, lane = tid & 63, w = __builtin_amdgcn_readfirstlane(tid >> 6);
    const int kvh = c & 7, s = (c >> 3) & 7, b = c >> 6, grp = b * 8 + kvh;
    const int g = lane >> 4, r16 = lane & 15, half = lane >> 5, i32 = lane & 31;
    const __amdgpu_buffer_rsrc_t rs = __builtin_amdgcn_make_buffer_rsrc(p.ws, 0, WS_END, 0x00020000);
#define ST4(v, off) __builtin_amdgcn_raw_buffer_store_b128((v), rs, (off), 0, AUX_SC1)
#define ST2(v, off) __builtin_amdgcn_raw_buffer_store_b64((v), rs, (off), 0, AUX_SC1)
#define ST1(v, off) __builtin_amdgcn_raw_buffer_store_b32((v), rs, (off), 0, AUX_SC1)
#define ST2S(v, voff, soff) __builtin_amdgcn_raw_buffer_store_b64((v), rs, (voff), (soff), AUX_SC1)
#define ST1S(v, voff, soff) __builtin_amdgcn_raw_buffer_store_b32((v), rs, (voff), (soff), AUX_SC1)
    KEEP_ACC();
    STAMP(0);
    unsigned tsv = 0; (void)tsv;
    unsigned tsx = 0; (void)tsx;
    XT(0);
    TS(0);
    const unsigned long long wsa = (unsigned long long)p.ws;
    const v4u rsv = {(unsigned)wsa, (unsigned)(wsa >> 32) & 0xFFFFu, (unsigned)WS_END, 0x00020000u};

    auto stage_w = [&](const u16* wmat, const unsigned ld, const unsigned coloff, const int u, const unsigned ldsb, const int h) {
        const unsigned ln = opq_v((unsigned)lane);
        const unsigned wrow = ln >> 3, wpos = ln & 7, wf = (wrow >> 1) & 1;
        const unsigned v0 = wrow * ld + ((((wpos >> 1) ^ wf) << 1) | (wpos & 1)) * 16;
        const unsigned v1 = wrow * ld + ((((wpos >> 1) ^ (wf | 2)) << 1) | (wpos & 1)) * 16;
        const unsigned char* wb = (const unsigned char*)(opq_s(wmat) + coloff) + (size_t)(u * 128 + h * 64) * ld;
        const int bb = opq_s(b);
#pragma unroll 1
        for (int i = 0; i < 8; i += 2) {
            const int pc = (i + 2 * bb) & 7;
            dma16(wb + (size_t)(pc * 8) * ld, v0, ldsb + h * 8192 + pc * 1024);
            dma16(wb + (size_t)(pc * 8 + 8) * ld, v1, ldsb + h * 8192 + pc * 1024 + 1024);
        }
    };
    const unsigned kvrow = (unsigned)((b * HKV + kvh) * SK + (SK - W) + s * 128);
    auto stage_k = [&](const u16* cache, const int u, const unsigned ldsb, const int h0, const int h1) {
        const unsigned ln = opq_v((unsigned)lane), gg = ln >> 4, rr = ln & 15;
        const unsigned char* sb = (const unsigned char*)opq_s(cache) + (size_t)(kvrow + u * 32) * 256;
        const unsigned v0 = gg * 256 + (rr ^ gg) * 16, v1 = gg * 256 + (rr ^ (4 + gg)) * 16, v2 = gg * 256 + (rr ^ (8 + gg)) * 16, v3 = gg * 256 + (rr ^ (12 + gg)) * 16;
        if (h0 == 0) dma16x4(sb, v0, v1, v2, v3, ldsb);
        if (h1 == 2) dma16x4(sb + 4096, v0, v1, v2, v3, ldsb + 4096);
    };
    auto stage_v = [&](const u16* cache, const int u, const unsigned ldsb) {
        const unsigned ln = opq_v((unsigned)lane), gg = ln >> 4, rr = ln & 15;
        const unsigned char* sb = (const unsigned char*)opq_s(cache) + (size_t)(kvrow + u * 32) * 256;
        const unsigned v0 = gg * 256 + ((((rr >> 1) ^ gg) << 1) | (rr & 1)) * 16, v1 = gg * 256 + ((((rr >> 1) ^ (4 + gg)) << 1) | (rr & 1)) * 16;
        dma16x4(sb, v0, v1, v0, v1, ldsb);
        dma16x4(sb + 4096, v0, v1, v0, v1, ldsb + 4096);
    };
    auto consume_w = [&](auto NC, const unsigned ldsb) {
        constexpr int N = decltype(NC)::value;
        const unsigned ln = opq_v((unsigned)lane), gg = ln >> 4, qq = (ln & 15) >> 2, pp = ln & 3;
        const unsigned f = (qq >> 1) | ((gg & 1) << 1);
        const unsigned base = ldsb + (8 * gg + qq) * 128 + (pp >> 1) * 16 + (pp & 1) * 8;
        tr_col<N>(base + (0 ^ f) * 32); tr_col<N + 16>(base + (1 ^ f) * 32); tr_col<N + 32>(base + (2 ^ f) * 32); tr_col<N + 48>(base + (3 ^ f) * 32);
        asm volatile("s_waitcnt lgkmcnt(0)" ::: "memory");
    };
#define LOAD_K(R, cache) do { \
        const unsigned ln_ = opq_v((unsigned)lane); \
        const unsigned char* kb_ = (const unsigned char*)opq_s(cache) + (size_t)(kvrow + opq_s(w) * 32) * 256; \
        const unsigned v_ = (ln_ & 15) * 256 + (ln_ >> 4) * 16, v2_ = v_ + 4096; \
        asm volatile("s_nop 4\n\tglobal_load_dwordx4 %0, %8, %10\n\tglobal_load_dwordx4 %1, %8, %10 offset:64\n\tglobal_load_dwordx4 %2, %8, %10 offset:128\n\tglobal_load_dwordx4 %3, %8, %10 offset:192\n\t" \
                     "global_load_dwordx4 %4, %9, %10\n\tglobal_load_dwordx4 %5, %9, %10 offset:64\n\tglobal_load_dwordx4 %6, %9, %10 offset:128\n\tglobal_load_dwordx4 %7, %9, %10 offset:192" \
                     : "=&v"(R.kf[0]), "=&v"(R.kf[1]), "=&v"(R.kf[2]), "=&v"(R.kf[3]), "=&v"(R.kf[4]), "=&v"(R.kf[5]), "=&v"(R.kf[6]), "=&v"(R.kf[7]) \
                     : "v"(v_), "v"(v2_), "s"(kb_) : "memory"); } while (0)
#define PIN_K(R) asm volatile("; COVERED_K" : "+v"(R.kf[0]), "+v"(R.kf[1]), "+v"(R.kf[2]), "+v"(R.kf[3]), "+v"(R.kf[4]), "+v"(R.kf[5]), "+v"(R.kf[6]), "+v"(R.kf[7]))
    auto consume_k = [&](v4u* dst, const unsigned ldsb) {
        const unsigned ln = opq_v((unsigned)lane), gg = ln >> 4, rr = ln & 15;
#pragma unroll
        for (int t = 0; t < 4; ++t) {
            LAS unsigned char* a = L + ldsb + rr * 256 + (((4 * t + gg) ^ rr) * 16);
            dst[t] = *(LAS v4u*)a; dst[4 + t] = *(LAS v4u*)(a + 4096);
        }
    };
    auto consume_v = [&](v4u* dst, const unsigned off) {
        const unsigned ln = opq_v((unsigned)lane), gg = ln >> 4, qq = (ln & 15) >> 2, pp = ln & 3;
        const unsigned vrow = 4 * gg + qq;
#pragma unroll
        for (int i = 0; i < 2; ++i) {
            LAS unsigned char* a = L + off + vrow * 256 + (((unsigned)(2 * w + i) ^ (vrow & 7)) * 32) + (pp >> 1) * 16 + (pp & 1) * 8;
#pragma unroll
            for (int t = 0; t < 4; ++t) dst[t * 2 + i] = tr2(a + t * RW, a + t * RW + 4096);
        }
    };
    const unsigned wq_col = (unsigned)(kvh * 512 + s * 64), wo_col = (unsigned)(kvh * 512 * 512 + s * 64);
    const unsigned myr = (unsigned)(w * RW);

    Res RA, RB;
#define RC(x)

    const unsigned xcol = (unsigned)(128 * w + 8 * r16 + 4 * (g >> 1) + 2 * (g & 1));
    unsigned xpk = 0;
    v2u gen = {0u, 0u};
    if (CODE_KB && w == 3) {
        const int piece = (c >> 3) % CODE_KB;
        dma16c((const unsigned char*)(entry_pc & ~255ull) + piece * 1024, (unsigned)lane * 16, L_TRASH);
    }
    if (w == 0) {
        dma16s(p.ws + WS_GEN, opq_v((unsigned)lane) * 16, L_GENB);
        dma16((const unsigned char*)p.x + b * 1024, opq_v((unsigned)lane) * 16, L_XS);
    }
    stage_w(p.wq0, 8192, wq_col, w, myr + U0, 0); stage_w(p.wq0, 8192, wq_col, w, myr + U0, 1);
    TS(15);
    LOAD_K(RA, p.kc0);
    stage_v(p.vc0, w, myr + U1 + 8192);
    TS(30);
    TS(45);
    ((LAS unsigned*)(L + L_ON))[lane] = 0x3F803F80u;

    const unsigned xg_v0 = (unsigned)(g * XB_K + w * 256 + r16 * 16), xg_v1 = xg_v0 + 4 * XB_K;
    const unsigned vq_st = ((lane & 1) == 0 && g == w) ? (unsigned)(lane * 2) : OOB;
    const unsigned vo_st = r16 < 4 ? (unsigned)(r16 * 256 + g * 8) : OOB;
    const unsigned vl_st = (lane < 4) ? (unsigned)(lane * 32) : OOB;
    const unsigned vx_st = (unsigned)((2 * w + half) * XB_R + i32 * 4);

    auto x_sum = [&](const v4u& a0, const v4u& a1, const bool active, float& o0, float& o1) -> bool {
        float v[8];
#pragma unroll
        for (int j = 0; j < 4; ++j) { v[2 * j] = XLO(a1[j], bflo(a0[j])); v[2 * j + 1] = XHI(a1[j], bfhi(a0[j])); }
        float u[4];
#pragma unroll
        for (int j = 0; j < 4; ++j) u[j] = swap32_add(v[j], v[j + 4]);
        o0 = swap16_add(u[0], u[2]); o1 = swap16_add(u[1], u[3]);
        const bool ok = !active || !__builtin_isunordered(o0, o1);
        if (__all(ok)) return true;
        const unsigned m = max(mx4(a0), mx4(a1));
        return __all(!active || m != EMPTY);
    };
    auto hello_ok = [&](const v2u& h) -> bool { return __all(h.x == gen.x && h.y == gen.y); };
    bool hello64 = false;

#pragma unroll 1
    for (int lp = 0; lp < NLAY; ++lp) {
        const int l = opq_s(lp < 0 ? 0 : lp);
        const bool live = lp >= 0;
        const bool TA = (lp & 1) == 0;
        const bool f0 = lp == 0, f1 = lp == 1;
        const int so_q = WS_QB + l * QB_L + grp * 1024, so_o = WS_OB + l * OB_L + grp * OB_G, so_l = WS_LB + l * LB_L + grp * 128, so_x = WS_XB + l * XB_L + b * XB_B;
        if (f0) {
            TS(60);
            VMWAIT(16);
            BAR();
            {
                const v2u g0 = *(LAS v2u*)(L + L_GENB + b * 256);
                const unsigned long long g1 = (((unsigned long long)g0.y << 32) | g0.x) + 1ull;
                gen = v2u{(unsigned)g1, (unsigned)(g1 >> 32)};
            }
            xpk = *(LAS unsigned*)(L + L_XS + xcol * 2);
        }
        STAMP(1 + l * 8 + 0);
        if (f0) XT(4);
        if (lp == 2) XT(28);
        KEEP_ACC();
        TS(1 + l * 15 + 0);
        if (lp > 0) *(LAS unsigned*)(L + L_XS + xcol * 2) = xpk;
        float rstd, tq;
        f32x4 acc[4];
        {
            LAS unsigned char* xb = L + (((f0 || f1) && r16 == 1) ? L_ON : L_XS + w * 256) + g * 16;
            v4u f[4];
#pragma unroll
            for (int t = 0; t < 4; ++t) f[t] = *(LAS v4u*)(xb + t * 64);
            SB();
            {
                const float x0 = bflo(xpk), x1 = bfhi(xpk);
                float p1 = x0 + x1, p2 = x0 * x0 + x1 * x1;
                dpp_acc<XOR1>(p1); dpp_acc<XOR1>(p2); dpp_acc<XOR2>(p1); dpp_acc<XOR2>(p2);
                dpp_acc<HMIR>(p1); dpp_acc<HMIR>(p2); dpp_acc<MIR>(p1); dpp_acc<MIR>(p2);
                *(LAS f32x2*)(L + L_ST + (w * 4 + g) * 8) = f32x2{p1, p2};
            }
            SB();
            if (f0) consume_w(IC<0>{}, myr + U0);
            TS(1 + l * 15 + 1);
            const unsigned a_st = (unsigned)(L_ST + r16 * 8);
            const int livei = opq_s(lp);
            if (TA) QBLOCK(0); else QBLOCK(128);
            LAS float* rq = (LAS float*)(L + L_RQ) + w * 64 + r16;
            rq[0] = acc[0][0]; rq[16] = acc[1][0]; rq[32] = acc[2][0]; rq[48] = acc[3][0];
            if (f0 || f1) {
                LAS float* rc = (LAS float*)(L + (g == 0 ? L_RC + w * 256 : L_TRASH + g * 256)) + r16;
                rc[0] = acc[0][1]; rc[16] = acc[1][1]; rc[32] = acc[2][1]; rc[48] = acc[3][1];
            }
        }
        asm volatile("s_waitcnt lgkmcnt(0)" ::: "memory");
        if (live) __builtin_amdgcn_s_barrier();
        asm volatile("" ::: "memory");
        TS(1 + l * 15 + 2);
        KEEP_ACC();
        v4u qa0 = {0u, 0u, 0u, 0u};
        {
            LAS float* rq = (LAS float*)(L + L_RQ) + lane;
            const float raw = (rq[0] + rq[64]) + (rq[128] + rq[192]);
            if (f0 || f1) {
                LAS float* rc = (LAS float*)(L + L_RC) + lane;
                const float cs = (rc[0] + rc[64]) + (rc[128] + rc[192]);
                if (f0) RA.cs = cs; else RB.cs = cs;
            }
            const float q = raw * rstd + tq * ((lp & 1) ? RB.cs : RA.cs);
            const unsigned qo = noempty(cvtpk(q, dpp<XOR1>(q)));
            if (live) {
                const int soff = opq_s(so_q + w * 256);
                const unsigned voff = (unsigned)(r16 * 16);
                if (f0) {
                    ST2S((v2u{qo, gen.x}), vq_st == OOB ? OOB : (unsigned)((lane >> 1) * 8), WS_QT + grp * 2048 + s * 256);
                    XT(5);
                    TS(1 + l * 15 + 3);
                    stage_w(p.wo0, 1024, wo_col, w, myr + U0, 0); stage_w(p.wo0, 1024, wo_col, w, myr + U0, 1);
                    {
                        const v4u e = {EMPTY, EMPTY, EMPTY, EMPTY};
                        const unsigned ln = opq_v((unsigned)lane);
#pragma unroll
                        for (int l4 = 0; l4 < NLAY; l4 += 4) {
                            ST4(e, WS_OB + (l4 + w) * OB_L + grp * OB_G + s * OB_P + ln * 16);
                            ST4(e, WS_XB + (l4 + w) * XB_L + b * XB_B + (ln >> 3) * XB_R + kvh * XB_K + s * 128 + (ln & 7) * 16);
                        }
                        if (w == 0) ST4(e, (ln >> 3) < NLAY ? (unsigned)(WS_QB + (ln >> 3) * QB_L + grp * 1024 + s * 128 + (ln & 7) * 16) : OOB);
                        if (w == 1) ST1(EMPTY, (ln >> 2) < NLAY ? (unsigned)(WS_LB + (ln >> 2) * LB_L + grp * 128 + (ln & 3) * 32 + s * 4) : OOB);
                    }
                    XT(6);
                    const int sofft = opq_s(WS_QT + grp * 2048 + w * 512);
                    const unsigned vofft = (unsigned)(i32 * 16);
                    v4u t;
                    BLD4(t, vofft, sofft, 0);
                    LOAD_K(RB, p.kc1);
                    stage_v(p.vc1, w, myr + U1);
                    asm volatile("s_waitcnt vmcnt(16) ; COVER" : "+v"(t) :: "memory");
                    PIN_K(RA);
                    if (UNLIKELY(!__all(t.y == gen.x && t.w == gen.x))) { for (unsigned sp = 0; sp < SPIN_CAP; ++sp) { BLD4W(t, vofft, sofft); if (__all(t.y == gen.x && t.w == gen.x)) break; } }
                    *(LAS v2u*)(L + L_QS + w * 256 + i32 * 8) = v2u{t.x, t.z};
                    XT(8);
                } else {
                    ST1S(qo, vq_st, so_q + s * 128);
                    TS(1 + l * 15 + 3);
                    delay<DLY_Q>();
                    BLD4W(qa0, voff, soff);
                }
                if (!f0 && UNLIKELY(!__all(mx4(qa0) != EMPTY))) {
                    for (unsigned sp = 0; sp < SPIN_CAP; ++sp) { RC(rcq); BLD4W(qa0, voff, soff); if (__all(mx4(qa0) != EMPTY)) break; }
                }
            }
        }
        TS(1 + l * 15 + 4);
        KEEP_ACC();
        if (!f0) *(LAS v4u*)(L + L_QS + w * 256 + r16 * 16) = qa0;
        STAMP(1 + l * 8 + 2);
        asm volatile("s_waitcnt lgkmcnt(0)" ::: "memory");
        if (live) __builtin_amdgcn_s_barrier();
        asm volatile("" ::: "memory");
        TS(1 + l * 15 + 5);
        if (f0 && tid == 0) ST2(gen, WS_HELLO + (b * 64 + kvh * 8 + s) * 8);
        f32x4 sc0, sc1;
        {
            LAS unsigned char* qb = L + L_QS + (lane & 3) * 256 + g * 16;
            v4u qf[4];
#pragma unroll
            for (int t = 0; t < 4; ++t) qf[t] = *(LAS v4u*)(qb + t * 64);
            SB();
            if (TA) QKBLOCK(RA); else QKBLOCK(RB);
            SB();
            if (f0) consume_v(RA.vf, U1 + 8192);
            asm volatile("s_nop 7" : "+v"(sc0), "+v"(sc1));
        }
        TS(1 + l * 15 + 6);
        KEEP_ACC();
        constexpr float CS = 0.08838834764831845f * 1.4426950408889634f;
        float mw = max3f(max3f(sc0[0], sc0[1], sc0[2]), max3f(sc0[3], sc1[0], sc1[1]), max2f(sc1[2], sc1[3]));
        mw = x16_maxf(mw) * CS;
        float pe[8];
#pragma unroll
        for (int j = 0; j < 4; ++j) { pe[j] = __builtin_amdgcn_exp2f(sc0[j] * CS - mw); pe[4 + j] = __builtin_amdgcn_exp2f(sc1[j] * CS - mw); }
        {
            const v4u pf = {cvtpk(pe[0], pe[1]), cvtpk(pe[2], pe[3]), cvtpk(pe[4], pe[5]), cvtpk(pe[6], pe[7])};
            *(LAS v4u*)(L + L_PS + w * 256 + (lane & 3) * 64 + g * 16) = pf;
            SB();
            float lw = ((pe[0] + pe[1]) + (pe[2] + pe[3])) + ((pe[4] + pe[5]) + (pe[6] + pe[7]));
            lw = x16_sum(lw);
            *(LAS f32x2*)(L + L_ML + (lane & 3) * 32 + w * 8) = f32x2{mw, lw};
        }
        TS(1 + l * 15 + 7);
        KEEP_ACC();
        asm volatile("s_waitcnt lgkmcnt(0)" ::: "memory");
        if (live) __builtin_amdgcn_s_barrier();
        asm volatile("" ::: "memory");
        v4u oa0 = {0u, 0u, 0u, 0u}, oa1 = oa0; unsigned la = 0;
        {
            LAS unsigned char* pb = L + L_PS + (lane & 3) * 64 + g * 16;
            const f32x4 ml0 = *(LAS f32x4*)(L + L_ML + (lane & 3) * 32), ml1 = *(LAS f32x4*)(L + L_ML + (lane & 3) * 32 + 16);
            v4u pf[4];
#pragma unroll
            for (int t = 0; t < 4; ++t) pf[t] = *(LAS v4u*)(pb + t * 256);
            SB();
            const float M = max2f(max3f(ml0[0], ml0[2], ml1[0]), ml1[2]);
            const float a0 = __builtin_amdgcn_exp2f(ml0[0] - M), a1 = __builtin_amdgcn_exp2f(ml0[2] - M);
            const float a2 = __builtin_amdgcn_exp2f(ml1[0] - M), a3 = __builtin_amdgcn_exp2f(ml1[2] - M);
            const float Ls = (a0 * ml0[1] + a1 * ml0[3]) + (a2 * ml1[1] + a3 * ml1[3]);
            const float inv = __builtin_amdgcn_rcpf(Ls);
            float lam = M + __builtin_amdgcn_logf(Ls);
            float b0 = a0 * inv, b1 = a1 * inv, b2 = a2 * inv, b3 = a3 * inv;
            PIN(b0); PIN(b1); PIN(b2); PIN(b3); PIN(lam);
            SB();
            f32x4 oa[4][2];
            if (TA) PVBLOCK(RA); else PVBLOCK(RB);
            TS(1 + l * 15 + 8);
            f32x4 o0 = oa[0][0] * b0, o1 = oa[0][1] * b0;
            o0 += oa[1][0] * b1; o1 += oa[1][1] * b1;
            o0 += oa[2][0] * b2; o1 += oa[2][1] * b2;
            o0 += oa[3][0] * b3; o1 += oa[3][1] * b3;
            const v2u w0 = {noempty(cvtpk(o0[0], o0[1])), noempty(cvtpk(o0[2], o0[3]))};
            const v2u w1 = {noempty(cvtpk(o1[0], o1[1])), noempty(cvtpk(o1[2], o1[3]))};
            if (live) {
                const int ob = so_o + s * OB_P;
                ST2S(w0, vo_st, ob + w * 64);
                ST2S(w1, vo_st, ob + w * 64 + 32);
                if (w == 0) ST1S(noempty(__float_as_uint(lam)), vl_st, so_l + s * 4);
                STAMP(1 + l * 8 + 3);
                TS(1 + l * 15 + 9);
                const int soff = opq_s(so_o + w * 256), soffl = opq_s(so_l + w * 32);
                const unsigned voff = (unsigned)((r16 & 7) * OB_P + (r16 >> 3) * 64 + g * 16);
                const unsigned voffl = (unsigned)((lane & 7) * 4);
                if (f0) {
                    v2u h;
                    const int hoff = opq_s(WS_HELLO + (b * 64) * 8);
                    const unsigned hv = (unsigned)(lane * 8);
                    const bool mine = (lane >> 3) == kvh;
                    BLD2(h, hv, hoff, 0);
                    consume_w(IC<64>{}, myr + U0);
                    stage_w(p.wq1, 8192, wq_col, w, myr + U0, 0); stage_w(p.wq1, 8192, wq_col, w, myr + U0, 1);
                    asm volatile("s_waitcnt vmcnt(16) ; COVER" : "+v"(h) :: "memory");
                    PIN_K(RB);
                    BAR();
                    consume_v(RB.vf, U1);
                    BAR();
                    if (UNLIKELY(!__all(!mine || (h.x == gen.x && h.y == gen.y)))) { for (unsigned sp = 0; sp < SPIN_CAP; ++sp) { BLD2W(h, hv, hoff); if (__all(!mine || (h.x == gen.x && h.y == gen.y))) break; } }
                    hello64 = hello_ok(h);
                    BLD4(oa0, voff, soff, 0); BLD4(oa1, voff, soff, 128); BLD1(la, voffl, soffl, 0);
                    stage_w(p.wo1, 1024, wo_col, w, myr + U1, 0); stage_w(p.wo1, 1024, wo_col, w, myr + U1, 1);
                    asm volatile("s_waitcnt vmcnt(16) ; COVER" : "+v"(oa0), "+v"(oa1), "+v"(la) :: "memory");
                } else if (f1) {
                    delay<DLY_O - 2>();
                    consume_w(IC<192>{}, myr + U1);
                    BLDOW(oa0, oa1, la, voff, voffl, soff, soffl);
                } else {
                    delay<DLY_O>();
                    BLDOW(oa0, oa1, la, voff, voffl, soff, soffl);
                }
                if (UNLIKELY(!__all(max(max(mx4(oa0), mx4(oa1)), la) != EMPTY))) {
                    for (unsigned sp = 0; sp < SPIN_CAP; ++sp) { RC(rco); BLDOW(oa0, oa1, la, voff, voffl, soff, soffl); if (__all(max(max(mx4(oa0), mx4(oa1)), la) != EMPTY)) break; }
                }
            }
        }
        STAMP(1 + l * 8 + 4);
        TS(1 + l * 15 + 10);
        KEEP_ACC();
        {
            f32x4 aL[4], aH[4];
            f32x4 wt; float tm, te;
            const unsigned a_wtw = (unsigned)(L_WT + w * 32 + (lane & 7) * 4), a_wtr = (unsigned)(L_WT + w * 32 + (g & 1) * 16);
            if (TA) OBLOCK(64); else OBLOCK(192);
            const f32x2 z2 = {0.f, 0.f};
            const f32x2 wL0 = half ? z2 : f32x2{wt[0], wt[1]}, wL1 = half ? z2 : f32x2{wt[2], wt[3]};
            const f32x2 wH0 = half ? f32x2{wt[0], wt[1]} : z2, wH1 = half ? f32x2{wt[2], wt[3]} : z2;
            TS(1 + l * 15 + 11);
            float v[4];
#pragma unroll
            for (int n = 0; n < 4; ++n) {
                f32x2 t = f32x2{aL[n][0], aL[n][1]} * wL0;
                t += f32x2{aL[n][2], aL[n][3]} * wL1; t += f32x2{aH[n][0], aH[n][1]} * wH0; t += f32x2{aH[n][2], aH[n][3]} * wH1;
                v[n] = t.x + t.y;
            }
            const float u0 = swap32_add(v[0], v[1]), u1 = swap32_add(v[2], v[3]);
            const float tot = swap16_add(u0, u1);
            *((LAS float*)(L + L_RX) + w * 64 + ((g & 1) * 2 + (g >> 1)) * 16 + r16) = tot;
        }
        asm volatile("s_waitcnt lgkmcnt(0)" ::: "memory");
        if (live) __builtin_amdgcn_s_barrier();
        asm volatile("" ::: "memory");
        TS(1 + l * 15 + 12);
        KEEP_ACC();
        {
            LAS unsigned char* rx = L + L_RX + i32 * 8;
            const f32x2 sum = (*(LAS f32x2*)(rx) + *(LAS f32x2*)(rx + 256)) + (*(LAS f32x2*)(rx + 512) + *(LAS f32x2*)(rx + 768));
            const unsigned ov = noempty(cvtpk(sum.x, sum.y));
            if (live) {
                ST1S(ov, vx_st, so_x + kvh * XB_K + s * 128);
                STAMP(1 + l * 8 + 5);
                TS(1 + l * 15 + 13);
                const bool fin = lp == NLAY - 1;
                const bool act = !fin || (s == 0 && w == (kvh >> 1) && (r16 >> 3) == (kvh & 1));
                if (!fin || (s == 0 && w == (kvh >> 1))) {
                    const int soff = opq_s(so_x + kvh * XB_R);
                    const unsigned v0 = act ? xg_v0 : OOB, v1 = act ? xg_v1 : OOB;
                    v4u a0, a1;
                    if (f0) {
                        XT(14);
                        if (UNLIKELY(!hello64)) {
                            v2u h;
                            const int hoff = opq_s(WS_HELLO + (b * 64) * 8);
                            const unsigned hv = (unsigned)(lane * 8);
                            for (unsigned sp = 0; sp < SPIN_CAP; ++sp) { BLD2W(h, hv, hoff); if (hello_ok(h)) break; }
                        }
                        if ((c & 63) == 0 && tid == 0) ST2(gen, WS_GEN + b * 256);
                        consume_w(IC<128>{}, myr + U0);
                        delay<DLY_X - 4>();
                        BLDXW(a0, a1, v0, v1, soff);
                        XT(17);
                    } else {
                        delay<DLY_X>();
                        BLDXW(a0, a1, v0, v1, soff);
                    }
                    float o0, o1;
                    if (UNLIKELY(!x_sum(a0, a1, act, o0, o1))) {
                        for (unsigned sp = 0; sp < SPIN_CAP; ++sp) { RC(rcx); BLDXW(a0, a1, v0, v1, soff); if (x_sum(a0, a1, act, o0, o1)) break; }
                    }
                    xpk = cvtpk(bflo(xpk) + o0, bfhi(xpk) + o1);
                    if (f0) XT(18);
                    if (f1) XT(27);
                    if (fin && act) *(GAS unsigned*)((GAS unsigned char*)p.out + (b * 512 + xcol) * 2) = xpk;
                }
            }
        }
    }
    STAMP(40);
}
}

extern "C" void kernel_launch(void* const* d_in, const int* in_sizes, int n_in, void* d_out, int out_size, void* d_ws, size_t ws_size, hipStream_t stream) {
    mk::Params p;
    p.x = (const u16*)d_in[0]; p.kc0 = (const u16*)d_in[1]; p.vc0 = (const u16*)d_in[2]; p.wq0 = (const u16*)d_in[3]; p.wo0 = (const u16*)d_in[4];
    p.kc1 = (const u16*)d_in[6]; p.vc1 = (const u16*)d_in[7]; p.wq1 = (const u16*)d_in[8]; p.wo1 = (const u16*)d_in[9];
    p.out = (u16*)d_out; p.ws = (unsigned char*)d_ws;
    (void)hipFuncSetAttribute((const void*)mk::mega, hipFuncAttributeMaxDynamicSharedMemorySize, mk::LDS_BYTES);
    hipLaunchKernelGGL(mk::mega, dim3(256), dim3(256), mk::LDS_BYTES, stream, p);
}
```

```cpp
#include <hip/hip_runtime.h>
#include <hip/hip_bf16.h>
#include <stdint.h>
#include <type_traits>

#define MK_STAMPS 0
#define NLAY 4
#define DRYRUN 0
#define PACE 63
#define CODE_KB 10
#define PRETOUCH 1
#define DLY_Q 6
#define DLY_O 6
#define DLY_X 14

#define GAS __attribute__((address_space(1)))
#define LAS __attribute__((address_space(3)))
typedef unsigned short u16;
typedef unsigned v4u __attribute__((ext_vector_type(4)));
typedef unsigned v2u __attribute__((ext_vector_type(2)));
typedef float f32x4 __attribute__((ext_vector_type(4)));
typedef float f32x2 __attribute__((ext_vector_type(2)));
typedef __bf16 bf16x8 __attribute__((ext_vector_type(8)));
typedef __bf16 bf16x2 __attribute__((ext_vector_type(2)));

namespace mk {
constexpr int HKV = 8, SK = 4096, DH = 128, W = 1024;
constexpr unsigned EMPTY = 0xFFFFFFFFu;
constexpr int AUX_SC1 = 16;
constexpr unsigned SPIN_CAP = 1u << 17;

constexpr int WS_GEN = 0;
constexpr int WS_HELLO = 1024;
constexpr int WS_QT = 4096;
constexpr int WS_QB = WS_QT + 32 * 2048;
constexpr int QB_L = 32 * 1024;
constexpr int WS_OB = WS_QB + NLAY * QB_L;
constexpr int OB_P = 1024, OB_G = 8 * OB_P, OB_L = 32 * OB_G;
constexpr int WS_LB = WS_OB + NLAY * OB_L;
constexpr int LB_L = 32 * 128;
constexpr int WS_XB = WS_LB + NLAY * LB_L;
constexpr int XB_K = 1024, XB_R = 8 * XB_K, XB_B = 8 * XB_R, XB_L = 4 * XB_B;
constexpr int WS_END = WS_XB + NLAY * XB_L;
constexpr int WS_STAMP = (WS_END + 4095) & ~4095;

constexpr int RW = 36864;
constexpr int U0 = 0, U1 = 16384;
constexpr int L_EX = 4 * RW;
constexpr int L_XS = L_EX;
constexpr int L_QS = L_XS + 1024;
constexpr int L_PS = L_QS + 1024;
constexpr int L_ON = L_PS + 1024;
constexpr int L_RQ = L_ON + 256;
constexpr int L_RC = L_RQ + 1024;
constexpr int L_RX = L_RC + 1024;
constexpr int L_ML = L_RX + 1024;
constexpr int L_ST = L_ML + 128;
constexpr int L_GEN = L_ST + 128;
constexpr int L_WT = L_GEN + 16;
constexpr int L_TRASH = L_WT + 128;
constexpr int L_GENB = L_TRASH + 1024;
constexpr int L_END = L_GENB + 1024;
constexpr int LDS_BYTES = 160 * 1024;
static_assert(L_END <= LDS_BYTES, "lds");
static_assert((L_WT % 16) == 0 && (L_ST % 16) == 0 && (L_RQ % 16) == 0 && (L_TRASH % 16) == 0, "align");

struct Params {
    const u16* x; const u16* kc0; const u16* vc0; const u16* wq0; const u16* wo0;
    const u16* kc1; const u16* vc1; const u16* wq1; const u16* wo1;
    u16* out; unsigned char* ws;
};

__device__ __forceinline__ f32x4 mfma(v4u a, v4u b, f32x4 c) {
    return __builtin_amdgcn_mfma_f32_16x16x32_bf16(__builtin_bit_cast(bf16x8, a), __builtin_bit_cast(bf16x8, b), c, 0, 0, 0);
}
__device__ __forceinline__ unsigned cvtpk(float lo, float hi) {
    bf16x2 r = {(__bf16)lo, (__bf16)hi};
    return __builtin_bit_cast(unsigned, r);
}
__device__ __forceinline__ float bflo(unsigned u) { return __builtin_bit_cast(float, u << 16); }
__device__ __forceinline__ float bfhi(unsigned u) { return __builtin_bit_cast(float, u & 0xFFFF0000u); }
template <int CTRL> __device__ __forceinline__ float dpp(float x) {
    return __builtin_bit_cast(float, __builtin_amdgcn_mov_dpp(__builtin_bit_cast(int, x), CTRL, 0xf, 0xf, true));
}
constexpr int XOR1 = 0xB1, XOR2 = 0x4E, HMIR = 0x141, MIR = 0x140, SHL1 = 0x101;
__device__ __forceinline__ float x16_max(float x) {
    auto s = __builtin_amdgcn_permlane16_swap(__float_as_uint(x), __float_as_uint(x), false, false);
    x = fmaxf(__uint_as_float(s[0]), __uint_as_float(s[1]));
    auto t = __builtin_amdgcn_permlane32_swap(__float_as_uint(x), __float_as_uint(x), false, false);
    return fmaxf(__uint_as_float(t[0]), __uint_as_float(t[1]));
}
__device__ __forceinline__ float x16_sum(float x) {
    auto s = __builtin_amdgcn_permlane16_swap(__float_as_uint(x), __float_as_uint(x), false, false);
    x = __uint_as_float(s[0]) + __uint_as_float(s[1]);
    auto t = __builtin_amdgcn_permlane32_swap(__float_as_uint(x), __float_as_uint(x), false, false);
    return __uint_as_float(t[0]) + __uint_as_float(t[1]);
}
__device__ __forceinline__ float row16_sum(float x) { x += dpp<XOR1>(x); x += dpp<XOR2>(x); x += dpp<HMIR>(x); x += dpp<MIR>(x); return x; }
__device__ __forceinline__ float row8_sum(float x) { x += dpp<XOR1>(x); x += dpp<XOR2>(x); x += dpp<HMIR>(x); return x; }
__device__ __forceinline__ float row8_max(float x) { x = fmaxf(x, dpp<XOR1>(x)); x = fmaxf(x, dpp<XOR2>(x)); x = fmaxf(x, dpp<HMIR>(x)); return x; }
__device__ __forceinline__ float swap32_add(float a, float b) {
    auto s = __builtin_amdgcn_permlane32_swap(__float_as_uint(a), __float_as_uint(b), false, false);
    return __uint_as_float(s[0]) + __uint_as_float(s[1]);
}
__device__ __forceinline__ float swap16_add(float a, float b) {
    auto s = __builtin_amdgcn_permlane16_swap(__float_as_uint(a), __float_as_uint(b), false, false);
    return __uint_as_float(s[0]) + __uint_as_float(s[1]);
}
__device__ __forceinline__ unsigned mx4(v4u v) { return max(max(v.x, v.y), max(v.z, v.w)); }
__device__ __forceinline__ unsigned noempty(unsigned u) { return min(u, 0xFFFFFFFEu); }
__device__ __forceinline__ f32x2 lo2(v4u v) { return f32x2{__uint_as_float(v.x), __uint_as_float(v.y)}; }
__device__ __forceinline__ f32x2 hi2(v4u v) { return f32x2{__uint_as_float(v.z), __uint_as_float(v.w)}; }

#define SB() __builtin_amdgcn_sched_barrier(0)
#define KEEP_ACC() asm volatile("" ::: "a0", "a1", "a2", "a3", "a4", "a5", "a6", "a7", "a8", "a9", "a10", "a11", "a12", "a13", "a14", "a15", "a16", "a17", "a18", "a19", "a20", "a21", "a22", "a23", "a24", "a25", "a26", "a27", "a28", "a29", "a30", "a31", "a32", "a33", "a34", "a35", "a36", "a37", "a38", "a39", "a40", "a41", "a42", "a43", "a44", "a45", "a46", "a47", "a48", "a49", "a50", "a51", "a52", "a53", "a54", "a55", "a56", "a57", "a58", "a59", "a60", "a61", "a62", "a63", "a64", "a65", "a66", "a67", "a68", "a69", "a70", "a71", "a72", "a73", "a74", "a75", "a76", "a77", "a78", "a79", "a80", "a81", "a82", "a83", "a84", "a85", "a86", "a87", "a88", "a89", "a90", "a91", "a92", "a93", "a94", "a95", "a96", "a97", "a98", "a99", "a100", "a101", "a102", "a103", "a104", "a105", "a106", "a107", "a108", "a109", "a110", "a111", "a112", "a113", "a114", "a115", "a116", "a117", "a118", "a119", "a120", "a121", "a122", "a123", "a124", "a125", "a126", "a127", "a128", "a129", "a130", "a131", "a132", "a133", "a134", "a135", "a136", "a137", "a138", "a139", "a140", "a141", "a142", "a143", "a144", "a145", "a146", "a147", "a148", "a149", "a150", "a151", "a152", "a153", "a154", "a155", "a156", "a157", "a158", "a159", "a160", "a161", "a162", "a163", "a164", "a165", "a166", "a167", "a168", "a169", "a170", "a171", "a172", "a173", "a174", "a175", "a176", "a177", "a178", "a179", "a180", "a181", "a182", "a183", "a184", "a185", "a186", "a187", "a188", "a189", "a190", "a191", "a192", "a193", "a194", "a195", "a196", "a197", "a198", "a199", "a200", "a201", "a202", "a203", "a204", "a205", "a206", "a207", "a208", "a209", "a210", "a211", "a212", "a213", "a214", "a215", "a216", "a217", "a218", "a219", "a220", "a221", "a222", "a223", "a224", "a225", "a226", "a227", "a228", "a229", "a230", "a231", "a232", "a233", "a234", "a235", "a236", "a237", "a238", "a239", "a240", "a241", "a242", "a243", "a244", "a245", "a246", "a247", "a248", "a249", "a250", "a251", "a252", "a253", "a254", "a255")
#define MFMA0(acc, A, B, CA) asm volatile("v_mfma_f32_16x16x32_bf16 %0, %1, %2, 0" : "=&v"(acc) : CA(A), "v"(B))
#define MFMA(acc, A, B, CA) asm volatile("v_mfma_f32_16x16x32_bf16 %0, %1, %2, %0" : "+v"(acc) : CA(A), "v"(B))
#define PIN(x) asm volatile("" : "+v"(x))
#define MFMAW0(acc, A, N) asm volatile("v_mfma_f32_16x16x32_bf16 %0, %1, a[%c2:%c3], 0" : "=&v"(acc) : "v"(A), "i"(N), "i"((N) + 3))
#define MFMAW(acc, A, N) asm volatile("v_mfma_f32_16x16x32_bf16 %0, %1, a[%c2:%c3], %0" : "+v"(acc) : "v"(A), "i"(N), "i"((N) + 3))
template <int N> __device__ __forceinline__ void tr_col(unsigned addr) {
    asm volatile("ds_read_b64_tr_b16 a[%c1:%c2], %0\n\tds_read_b64_tr_b16 a[%c3:%c4], %0 offset:512\n\t"
                 "ds_read_b64_tr_b16 a[%c5:%c6], %0 offset:4096\n\tds_read_b64_tr_b16 a[%c7:%c8], %0 offset:4608"
                 :: "v"(addr), "i"(N), "i"(N + 1), "i"(N + 2), "i"(N + 3), "i"(N + 4), "i"(N + 5), "i"(N + 6), "i"(N + 7) : "memory");
    asm volatile("ds_read_b64_tr_b16 a[%c1:%c2], %0 offset:8192\n\tds_read_b64_tr_b16 a[%c3:%c4], %0 offset:8704\n\t"
                 "ds_read_b64_tr_b16 a[%c5:%c6], %0 offset:12288\n\tds_read_b64_tr_b16 a[%c7:%c8], %0 offset:12800"
                 :: "v"(addr), "i"(N + 8), "i"(N + 9), "i"(N + 10), "i"(N + 11), "i"(N + 12), "i"(N + 13), "i"(N + 14), "i"(N + 15) : "memory");
}
#define QBLOCK(NB) asm volatile("v_mfma_f32_16x16x32_bf16 %[c0], %[f0], a[%c[N]+0:%c[N]+3], 0\n\t" \
        "v_mfma_f32_16x16x32_bf16 %[c1], %[f0], a[%c[N]+16:%c[N]+19], 0\n\t" \
        "s_waitcnt lgkmcnt(0)\n\t" \
        "s_cmp_ge_i32 %[live], 0\n\t" \
        "s_cbranch_scc0 1f\n\t" \
        "s_barrier\n\t" \
        "1:\n\t" \
        "ds_read_b32 %[p1], %[ast]\n\t" \
        "ds_read_b32 %[p2], %[ast] offset:4\n\t" \
        "v_mfma_f32_16x16x32_bf16 %[c2], %[f0], a[%c[N]+32:%c[N]+35], 0\n\t" \
        "v_mfma_f32_16x16x32_bf16 %[c3], %[f0], a[%c[N]+48:%c[N]+51], 0\n\t" \
        "v_mfma_f32_16x16x32_bf16 %[c0], %[f1], a[%c[N]+4:%c[N]+7], %[c0]\n\t" \
        "v_mfma_f32_16x16x32_bf16 %[c1], %[f1], a[%c[N]+20:%c[N]+23], %[c1]\n\t" \
        "s_waitcnt lgkmcnt(0)\n\t" \
        "v_add_f32_dpp %[p1], %[p1], %[p1] quad_perm:[1,0,3,2] row_mask:0xf bank_mask:0xf\n\t" \
        "v_add_f32_dpp %[p2], %[p2], %[p2] quad_perm:[1,0,3,2] row_mask:0xf bank_mask:0xf\n\t" \
        "v_mfma_f32_16x16x32_bf16 %[c2], %[f1], a[%c[N]+36:%c[N]+39], %[c2]\n\t" \
        "v_add_f32_dpp %[p1], %[p1], %[p1] quad_perm:[2,3,0,1] row_mask:0xf bank_mask:0xf\n\t" \
        "v_add_f32_dpp %[p2], %[p2], %[p2] quad_perm:[2,3,0,1] row_mask:0xf bank_mask:0xf\n\t" \
        "v_mfma_f32_16x16x32_bf16 %[c3], %[f1], a[%c[N]+52:%c[N]+55], %[c3]\n\t" \
        "v_add_f32_dpp %[p1], %[p1], %[p1] row_half_mirror row_mask:0xf bank_mask:0xf\n\t" \
        "v_add_f32_dpp %[p2], %[p2], %[p2] row_half_mirror row_mask:0xf bank_mask:0xf\n\t" \
        "v_mfma_f32_16x16x32_bf16 %[c0], %[f2], a[%c[N]+8:%c[N]+11], %[c0]\n\t" \
        "v_add_f32_dpp %[p1], %[p1], %[p1] row_mirror row_mask:0xf bank_mask:0xf\n\t" \
        "v_add_f32_dpp %[p2], %[p2], %[p2] row_mirror row_mask:0xf bank_mask:0xf\n\t" \
        "v_mfma_f32_16x16x32_bf16 %[c1], %[f2], a[%c[N]+24:%c[N]+27], %[c1]\n\t" \
        "v_mul_f32 %[p1], 0x3b000000, %[p1]\n\t" \
        "v_mul_f32 %[p2], 0x3b000000, %[p2]\n\t" \
        "v_mfma_f32_16x16x32_bf16 %[c2], %[f2], a[%c[N]+40:%c[N]+43], %[c2]\n\t" \
        "v_fma_f32 %[p2], -%[p1], %[p1], %[p2]\n\t" \
        "v_max_f32 %[p2], 0, %[p2]\n\t" \
        "v_mfma_f32_16x16x32_bf16 %[c3], %[f2], a[%c[N]+56:%c[N]+59], %[c3]\n\t" \
        "v_add_f32 %[p2], 0x3727c5ac, %[p2]\n\t" \
        "v_rsq_f32 %[p2], %[p2]\n\t" \
        "v_mfma_f32_16x16x32_bf16 %[c0], %[f3], a[%c[N]+12:%c[N]+15], %[c0]\n\t" \
        "v_mfma_f32_16x16x32_bf16 %[c1], %[f3], a[%c[N]+28:%c[N]+31], %[c1]\n\t" \
        "v_mul_f32 %[p1], %[p1], %[p2]\n\t" \
        "v_sub_f32 %[p1], 0, %[p1]\n\t" \
        "v_mfma_f32_16x16x32_bf16 %[c2], %[f3], a[%c[N]+44:%c[N]+47], %[c2]\n\t" \
        "v_mfma_f32_16x16x32_bf16 %[c3], %[f3], a[%c[N]+60:%c[N]+63], %[c3]\n\t" \
        "s_nop 7\n\t" "" \
        : [c0] "=&v"(acc[0]), [c1] "=&v"(acc[1]), [c2] "=&v"(acc[2]), [c3] "=&v"(acc[3]), [p1] "=&v"(tq), [p2] "=&v"(rstd) \
        : [f0] "v"(f[0]), [f1] "v"(f[1]), [f2] "v"(f[2]), [f3] "v"(f[3]), [ast] "v"(a_st), [live] "s"(livei), [N] "i"(NB) : "memory", "scc")
#define QKBLOCK(R) asm volatile("v_mfma_f32_16x16x32_bf16 %[s0], %[k0], %[q0], 0\n\t" \
        "v_mfma_f32_16x16x32_bf16 %[s1], %[k4], %[q0], 0\n\t" \
        "v_mfma_f32_16x16x32_bf16 %[s0], %[k1], %[q1], %[s0]\n\t" \
        "v_mfma_f32_16x16x32_bf16 %[s1], %[k5], %[q1], %[s1]\n\t" \
        "v_mfma_f32_16x16x32_bf16 %[s0], %[k2], %[q2], %[s0]\n\t" \
        "v_mfma_f32_16x16x32_bf16 %[s1], %[k6], %[q2], %[s1]\n\t" \
        "v_mfma_f32_16x16x32_bf16 %[s0], %[k3], %[q3], %[s0]\n\t" \
        "v_mfma_f32_16x16x32_bf16 %[s1], %[k7], %[q3], %[s1]\n\t" "" \
        : [s0] "=&v"(sc0), [s1] "=&v"(sc1) \
        : [k0] "v"(R.kf[0]), [k1] "v"(R.kf[1]), [k2] "v"(R.kf[2]), [k3] "v"(R.kf[3]), [k4] "v"(R.kf[4]), [k5] "v"(R.kf[5]), [k6] "v"(R.kf[6]), [k7] "v"(R.kf[7]), [q0] "v"(qf[0]), [q1] "v"(qf[1]), [q2] "v"(qf[2]), [q3] "v"(qf[3]))
#define PVBLOCK(R) asm volatile("v_mfma_f32_16x16x32_bf16 %[o0], %[v0], %[p0], 0\n\t" \
        "v_mfma_f32_16x16x32_bf16 %[o1], %[v1], %[p0], 0\n\t" \
        "v_mfma_f32_16x16x32_bf16 %[o2], %[v2], %[p1], 0\n\t" \
        "v_mfma_f32_16x16x32_bf16 %[o3], %[v3], %[p1], 0\n\t" \
        "v_mfma_f32_16x16x32_bf16 %[o4], %[v4], %[p2], 0\n\t" \
        "v_mfma_f32_16x16x32_bf16 %[o5], %[v5], %[p2], 0\n\t" \
        "v_mfma_f32_16x16x32_bf16 %[o6], %[v6], %[p3], 0\n\t" \
        "v_mfma_f32_16x16x32_bf16 %[o7], %[v7], %[p3], 0\n\t" \
        "s_nop 7\n\t" "" \
        : [o0] "=&v"(oa[0][0]), [o1] "=&v"(oa[0][1]), [o2] "=&v"(oa[1][0]), [o3] "=&v"(oa[1][1]), [o4] "=&v"(oa[2][0]), [o5] "=&v"(oa[2][1]), [o6] "=&v"(oa[3][0]), [o7] "=&v"(oa[3][1]) \
        : [v0] "v"(R.vf[0]), [v1] "v"(R.vf[1]), [v2] "v"(R.vf[2]), [v3] "v"(R.vf[3]), [v4] "v"(R.vf[4]), [v5] "v"(R.vf[5]), [v6] "v"(R.vf[6]), [v7] "v"(R.vf[7]), [p0] "v"(pf[0]), [p1] "v"(pf[1]), [p2] "v"(pf[2]), [p3] "v"(pf[3]))
#define OBLOCK(NB) asm volatile("v_mfma_f32_16x16x32_bf16 %[L0], %[a0], a[%c[N]+0:%c[N]+3], 0\n\t" \
        "v_max_f32_dpp %[m], %[la], %[la] quad_perm:[1,0,3,2] row_mask:0xf bank_mask:0xf\n\t" \
        "v_mfma_f32_16x16x32_bf16 %[H0], %[a0], a[%c[N]+4:%c[N]+7], 0\n\t" \
        "s_nop 0\n\t" \
        "v_max_f32_dpp %[m], %[m], %[m] quad_perm:[2,3,0,1] row_mask:0xf bank_mask:0xf\n\t" \
        "v_mfma_f32_16x16x32_bf16 %[L1], %[a0], a[%c[N]+16:%c[N]+19], 0\n\t" \
        "s_nop 0\n\t" \
        "v_max_f32_dpp %[m], %[m], %[m] row_half_mirror row_mask:0xf bank_mask:0xf\n\t" \
        "v_mfma_f32_16x16x32_bf16 %[H1], %[a0], a[%c[N]+20:%c[N]+23], 0\n\t" \
        "v_sub_f32 %[m], %[la], %[m]\n\t" \
        "v_exp_f32 %[e], %[m]\n\t" \
        "v_mfma_f32_16x16x32_bf16 %[L2], %[a0], a[%c[N]+32:%c[N]+35], 0\n\t" \
        "s_nop 0\n\t" \
        "v_add_f32_dpp %[m], %[e], %[e] quad_perm:[1,0,3,2] row_mask:0xf bank_mask:0xf\n\t" \
        "v_mfma_f32_16x16x32_bf16 %[H2], %[a0], a[%c[N]+36:%c[N]+39], 0\n\t" \
        "s_nop 0\n\t" \
        "v_add_f32_dpp %[m], %[m], %[m] quad_perm:[2,3,0,1] row_mask:0xf bank_mask:0xf\n\t" \
        "v_mfma_f32_16x16x32_bf16 %[L3], %[a0], a[%c[N]+48:%c[N]+51], 0\n\t" \
        "s_nop 0\n\t" \
        "v_add_f32_dpp %[m], %[m], %[m] row_half_mirror row_mask:0xf bank_mask:0xf\n\t" \
        "v_mfma_f32_16x16x32_bf16 %[H3], %[a0], a[%c[N]+52:%c[N]+55], 0\n\t" \
        "v_rcp_f32 %[m], %[m]\n\t" \
        "v_mfma_f32_16x16x32_bf16 %[L0], %[a1], a[%c[N]+8:%c[N]+11], %[L0]\n\t" \
        "v_mul_f32 %[e], %[e], %[m]\n\t" \
        "ds_write_b32 %[aw], %[e]\n\t" \
        "v_mfma_f32_16x16x32_bf16 %[H0], %[a1], a[%c[N]+12:%c[N]+15], %[H0]\n\t" \
        "ds_read_b128 %[wt], %[ar]\n\t" \
        "v_mfma_f32_16x16x32_bf16 %[L1], %[a1], a[%c[N]+24:%c[N]+27], %[L1]\n\t" \
        "v_mfma_f32_16x16x32_bf16 %[H1], %[a1], a[%c[N]+28:%c[N]+31], %[H1]\n\t" \
        "v_mfma_f32_16x16x32_bf16 %[L2], %[a1], a[%c[N]+40:%c[N]+43], %[L2]\n\t" \
        "v_mfma_f32_16x16x32_bf16 %[H2], %[a1], a[%c[N]+44:%c[N]+47], %[H2]\n\t" \
        "v_mfma_f32_16x16x32_bf16 %[L3], %[a1], a[%c[N]+56:%c[N]+59], %[L3]\n\t" \
        "v_mfma_f32_16x16x32_bf16 %[H3], %[a1], a[%c[N]+60:%c[N]+63], %[H3]\n\t" \
        "s_waitcnt lgkmcnt(0)\n\t" \
        "s_nop 6\n\t" "" \
        : [L0] "=&v"(aL[0]), [H0] "=&v"(aH[0]), [L1] "=&v"(aL[1]), [H1] "=&v"(aH[1]), [L2] "=&v"(aL[2]), [H2] "=&v"(aH[2]), [L3] "=&v"(aL[3]), [H3] "=&v"(aH[3]), \
          [wt] "=&v"(wt), [m] "=&v"(tm), [e] "=&v"(te) \
        : [a0] "v"(oa0), [a1] "v"(oa1), [la] "v"(la), [aw] "v"(a_wtw), [ar] "v"(a_wtr), [N] "i"(NB) : "memory")
__device__ __forceinline__ float max3f(float a, float b, float c) { float r; asm("v_max3_f32 %0, %1, %2, %3" : "=v"(r) : "v"(a), "v"(b), "v"(c)); return r; }
__device__ __forceinline__ float max2f(float a, float b) { float r; asm("v_max_f32 %0, %1, %2" : "=v"(r) : "v"(a), "v"(b)); return r; }
__device__ __forceinline__ float x16_maxf(float x) {
    auto s = __builtin_amdgcn_permlane16_swap(__float_as_uint(x), __float_as_uint(x), false, false);
    x = max2f(__uint_as_float(s[0]), __uint_as_float(s[1]));
    auto t = __builtin_amdgcn_permlane32_swap(__float_as_uint(x), __float_as_uint(x), false, false);
    return max2f(__uint_as_float(t[0]), __uint_as_float(t[1]));
}
constexpr unsigned OOB = 0x80000000u;
template <int CTRL> __device__ __forceinline__ void dpp_acc(float& x) { x += dpp<CTRL>(x); asm("" : "+v"(x)); }
template <int CTRL> __device__ __forceinline__ void dpp_mx(float& x) { x = max2f(x, dpp<CTRL>(x)); }
#define UNLIKELY(x) __builtin_expect(!!(x), 0)
#define BAR() do { asm volatile("s_waitcnt lgkmcnt(0)" ::: "memory"); __builtin_amdgcn_s_barrier(); asm volatile("" ::: "memory"); } while (0)
#define VMWAIT(n) asm volatile("s_waitcnt vmcnt(" #n ")" ::: "memory")
template <int N> __device__ __forceinline__ void delay() {
#pragma unroll
    for (int i = 0; i < N; ++i) asm volatile("s_nop 15");
}

__device__ __forceinline__ void dma16(const unsigned char* sbase, unsigned voff, unsigned lds) {
    unsigned keep;
    asm volatile("s_mov_b32 %0, m0\n\ts_mov_b32 m0, %3\n\ts_nop 2\n\tglobal_load_lds_dwordx4 %1, %2\n\ts_mov_b32 m0, %0\n\ts_waitcnt vmcnt(%c4)"
                 : "=&s"(keep) : "v"(voff), "s"(sbase), "s"(lds), "i"(PACE - 1) : "memory");
}
__device__ __forceinline__ void dma16c(const unsigned char* sbase, unsigned voff, unsigned lds) {
    unsigned keep;
    asm volatile("s_mov_b32 %0, m0\n\ts_mov_b32 m0, %3\n\ts_nop 2\n\tglobal_load_lds_dwordx4 %1, %2\n\ts_mov_b32 m0, %0"
                 : "=&s"(keep) : "v"(voff), "s"(sbase), "s"(lds) : "memory");
}
__device__ __forceinline__ void dma16s(const unsigned char* sbase, unsigned voff, unsigned lds) {
    unsigned keep;
    asm volatile("s_mov_b32 %0, m0\n\ts_mov_b32 m0, %3\n\ts_nop 2\n\tglobal_load_lds_dwordx4 %1, %2 sc1\n\ts_mov_b32 m0, %0"
                 : "=&s"(keep) : "v"(voff), "s"(sbase), "s"(lds) : "memory");
}
__device__ __forceinline__ void dma16x4(const unsigned char* sbase, unsigned v0, unsigned v1, unsigned v2, unsigned v3, unsigned lds) {
    unsigned keep;
    asm volatile("s_mov_b32 %0, m0\n\ts_mov_b32 m0, %6\n\ts_nop 2\n\t"
                 "global_load_lds_dwordx4 %1, %5\n\ts_waitcnt vmcnt(%c7)\n\tglobal_load_lds_dwordx4 %2, %5 offset:1024\n\ts_waitcnt vmcnt(%c7)\n\t"
                 "global_load_lds_dwordx4 %3, %5 offset:2048\n\ts_waitcnt vmcnt(%c7)\n\tglobal_load_lds_dwordx4 %4, %5 offset:3072\n\ts_waitcnt vmcnt(%c7)\n\ts_mov_b32 m0, %0"
                 : "=&s"(keep) : "v"(v0), "v"(v1), "v"(v2), "v"(v3), "s"(sbase), "s"(lds), "i"(PACE - 1) : "memory");
}
template <class T> __device__ __forceinline__ T opq_s(T v) { asm volatile("" : "+s"(v)); return v; }
template <class T> __device__ __forceinline__ T opq_v(T v) { asm volatile("" : "+v"(v)); return v; }
#define BLD4(dst, voff, soff, imm) asm volatile("buffer_load_dwordx4 %0, %1, %2, %3 offen offset:" #imm " sc1" : "=&v"(dst) : "v"(voff), "s"(rsv), "s"(soff) : "memory")
#define BLD2(dst, voff, soff, imm) asm volatile("buffer_load_dwordx2 %0, %1, %2, %3 offen offset:" #imm " sc1" : "=&v"(dst) : "v"(voff), "s"(rsv), "s"(soff) : "memory")
#define BLD1(dst, voff, soff, imm) asm volatile("buffer_load_dword %0, %1, %2, %3 offen offset:" #imm " sc1" : "=&v"(dst) : "v"(voff), "s"(rsv), "s"(soff) : "memory")
#define BLD4W(dst, voff, soff) asm volatile("buffer_load_dwordx4 %0, %1, %2, %3 offen sc1\n\ts_waitcnt vmcnt(0)" : "=&v"(dst) : "v"(voff), "s"(rsv), "s"(soff) : "memory")
#define BLD2W(dst, voff, soff) asm volatile("buffer_load_dwordx2 %0, %1, %2, %3 offen sc1\n\ts_waitcnt vmcnt(0)" : "=&v"(dst) : "v"(voff), "s"(rsv), "s"(soff) : "memory")
#define BLDOW(a0, a1, la, v0, v1, soff, soffl) asm volatile("buffer_load_dwordx4 %0, %3, %5, %6 offen sc1\n\tbuffer_load_dwordx4 %1, %3, %5, %6 offen offset:128 sc1\n\tbuffer_load_dword %2, %4, %5, %7 offen sc1\n\ts_waitcnt vmcnt(0)" \
    : "=&v"(a0), "=&v"(a1), "=&v"(la) : "v"(v0), "v"(v1), "s"(rsv), "s"(soff), "s"(soffl) : "memory")
#define BLDXW(a0, a1, v0, v1, soff) asm volatile("buffer_load_dwordx4 %0, %2, %4, %5 offen sc1\n\tbuffer_load_dwordx4 %1, %3, %4, %5 offen sc1\n\ts_waitcnt vmcnt(0)" \
    : "=&v"(a0), "=&v"(a1) : "v"(v0), "v"(v1), "s"(rsv), "s"(soff) : "memory")
#define XLO(a, c) (bflo(a) + (c))
#define XHI(a, c) (bfhi(a) + (c))
__device__ __forceinline__ float acc_lo(unsigned a, float c) { return __builtin_amdgcn_fdot2_f32_bf16(__builtin_bit_cast(bf16x2, a), __builtin_bit_cast(bf16x2, 0x00003F80u), c, false); }
__device__ __forceinline__ float acc_hi(unsigned a, float c) { return __builtin_amdgcn_fdot2_f32_bf16(__builtin_bit_cast(bf16x2, a), __builtin_bit_cast(bf16x2, 0x3F800000u), c, false); }

__device__ __forceinline__ v4u tr2(LAS unsigned char* p0, LAS unsigned char* p1) {
    typedef short v4i16_t __attribute__((ext_vector_type(4)));
    v4i16_t a = __builtin_amdgcn_ds_read_tr16_b64_v4i16((LAS v4i16_t*)p0);
    v4i16_t b = __builtin_amdgcn_ds_read_tr16_b64_v4i16((LAS v4i16_t*)p1);
    v2u x = __builtin_bit_cast(v2u, a), y = __builtin_bit_cast(v2u, b);
    return v4u{x.x, x.y, y.x, y.y};
}

#define STAMP(i) do {} while (0)

#define XT(i) do {} while (0)
#define TS(i) do {} while (0)
struct Res { v4u kf[8], vf[8]; float cs; };
template <int V> using IC = std::integral_constant<int, V>;

__global__ void __launch_bounds__(256, 1) mega(Params p) {
    unsigned long long entry_pc;
    asm volatile("s_getpc_b64 %0" : "=s"(entry_pc));
    extern __shared__ __attribute__((aligned(16))) unsigned char smem[];
    LAS unsigned char* L = (LAS unsigned char*)smem;
    const int c = blockIdx.x, tid = threadIdx.x, lane = tid & 63, w = __builtin_amdgcn_readfirstlane(tid >> 6);
    const int kvh = c & 7, s = (c >> 3) & 7, b = c >> 6, grp = b * 8 + kvh;
    const int g = lane >> 4, r16 = lane & 15, half = lane >> 5, i32 = lane & 31;
    const __amdgpu_buffer_rsrc_t rs = __builtin_amdgcn_make_buffer_rsrc(p.ws, 0, WS_END, 0x00020000);
#define ST4(v, off) __builtin_amdgcn_raw_buffer_store_b128((v), rs, (off), 0, AUX_SC1)
#define ST2(v, off) __builtin_amdgcn_raw_buffer_store_b64((v), rs, (off), 0, AUX_SC1)
#define ST1(v, off) __builtin_amdgcn_raw_buffer_store_b32((v), rs, (off), 0, AUX_SC1)
#define ST2S(v, voff, soff) __builtin_amdgcn_raw_buffer_store_b64((v), rs, (voff), (soff), AUX_SC1)
#define ST1S(v, voff, soff) __builtin_amdgcn_raw_buffer_store_b32((v), rs, (voff), (soff), AUX_SC1)
    KEEP_ACC();
    STAMP(0);
    unsigned tsv = 0; (void)tsv;
    unsigned tsx = 0; (void)tsx;
    XT(0);
    TS(0);
    const unsigned long long wsa = (unsigned long long)p.ws;
    const v4u rsv = {(unsigned)wsa, (unsigned)(wsa >> 32) & 0xFFFFu, (unsigned)WS_END, 0x00020000u};

    auto stage_w = [&](const u16* wmat, const unsigned ld, const unsigned coloff, const int u, const unsigned ldsb, const int h) {
        const unsigned ln = opq_v((unsigned)lane);
        const unsigned wrow = ln >> 3, wpos = ln & 7, wf = (wrow >> 1) & 1;
        const unsigned v0 = wrow * ld + ((((wpos >> 1) ^ wf) << 1) | (wpos & 1)) * 16;
        const unsigned v1 = wrow * ld + ((((wpos >> 1) ^ (wf | 2)) << 1) | (wpos & 1)) * 16;
        const unsigned char* wb = (const unsigned char*)(opq_s(wmat) + coloff) + (size_t)(u * 128 + h * 64) * ld;
        const int bb = opq_s(b);
#pragma unroll 1
        for (int i = 0; i < 8; i += 2) {
            const int pc = (i + 2 * bb) & 7;
            dma16(wb + (size_t)(pc * 8) * ld, v0, ldsb + h * 8192 + pc * 1024);
            dma16(wb + (size_t)(pc * 8 + 8) * ld, v1, ldsb + h * 8192 + pc * 1024 + 1024);
        }
    };
    const unsigned kvrow = (unsigned)((b * HKV + kvh) * SK + (SK - W) + s * 128);
    auto stage_k = [&](const u16* cache, const int u, const unsigned ldsb, const int h0, const int h1) {
        const unsigned ln = opq_v((unsigned)lane), gg = ln >> 4, rr = ln & 15;
        const unsigned char* sb = (const unsigned char*)opq_s(cache) + (size_t)(kvrow + u * 32) * 256;
        const unsigned v0 = gg * 256 + (rr ^ gg) * 16, v1 = gg * 256 + (rr ^ (4 + gg)) * 16, v2 = gg * 256 + (rr ^ (8 + gg)) * 16, v3 = gg * 256 + (rr ^ (12 + gg)) * 16;
        if (h0 == 0) dma16x4(sb, v0, v1, v2, v3, ldsb);
        if (h1 == 2) dma16x4(sb + 4096, v0, v1, v2, v3, ldsb + 4096);
    };
    auto stage_v = [&](const u16* cache, const int u, const unsigned ldsb) {
        const unsigned ln = opq_v((unsigned)lane), gg = ln >> 4, rr = ln & 15;
        const unsigned char* sb = (const unsigned char*)opq_s(cache) + (size_t)(kvrow + u * 32) * 256;
        const unsigned v0 = gg * 256 + ((((rr >> 1) ^ gg) << 1) | (rr & 1)) * 16, v1 = gg * 256 + ((((rr >> 1) ^ (4 + gg)) << 1) | (rr & 1)) * 16;
        dma16x4(sb, v0, v1, v0, v1, ldsb);
        dma16x4(sb + 4096, v0, v1, v0, v1, ldsb + 4096);
    };
    auto consume_w = [&](auto NC, const unsigned ldsb) {
        constexpr int N = decltype(NC)::value;
        const unsigned ln = opq_v((unsigned)lane), gg = ln >> 4, qq = (ln & 15) >> 2, pp = ln & 3;
        const unsigned f = (qq >> 1) | ((gg & 1) << 1);
        const unsigned base = ldsb + (8 * gg + qq) * 128 + (pp >> 1) * 16 + (pp & 1) * 8;
        tr_col<N>(base + (0 ^ f) * 32); tr_col<N + 16>(base + (1 ^ f) * 32); tr_col<N + 32>(base + (2 ^ f) * 32); tr_col<N + 48>(base + (3 ^ f) * 32);
        asm volatile("s_waitcnt lgkmcnt(0)" ::: "memory");
    };
#define LOAD_K(R, cache) do { \
        const unsigned ln_ = opq_v((unsigned)lane); \
        const unsigned char* kb_ = (const unsigned char*)opq_s(cache) + (size_t)(kvrow + opq_s(w) * 32) * 256; \
        const unsigned v_ = (ln_ & 15) * 256 + (ln_ >> 4) * 16, v2_ = v_ + 4096; \
        asm volatile("s_nop 4\n\tglobal_load_dwordx4 %0, %8, %10\n\tglobal_load_dwordx4 %1, %8, %10 offset:64\n\tglobal_load_dwordx4 %2, %8, %10 offset:128\n\tglobal_load_dwordx4 %3, %8, %10 offset:192\n\t" \
                     "global_load_dwordx4 %4, %9, %10\n\tglobal_load_dwordx4 %5, %9, %10 offset:64\n\tglobal_load_dwordx4 %6, %9, %10 offset:128\n\tglobal_load_dwordx4 %7, %9, %10 offset:192" \
                     : "=&v"(R.kf[0]), "=&v"(R.kf[1]), "=&v"(R.kf[2]), "=&v"(R.kf[3]), "=&v"(R.kf[4]), "=&v"(R.kf[5]), "=&v"(R.kf[6]), "=&v"(R.kf[7]) \
                     : "v"(v_), "v"(v2_), "s"(kb_) : "memory"); } while (0)
#define PIN_K(R) asm volatile("; COVERED_K" : "+v"(R.kf[0]), "+v"(R.kf[1]), "+v"(R.kf[2]), "+v"(R.kf[3]), "+v"(R.kf[4]), "+v"(R.kf[5]), "+v"(R.kf[6]), "+v"(R.kf[7]))
    auto consume_k = [&](v4u* dst, const unsigned ldsb) {
        const unsigned ln = opq_v((unsigned)lane), gg = ln >> 4, rr = ln & 15;
#pragma unroll
        for (int t = 0; t < 4; ++t) {
            LAS unsigned char* a = L + ldsb + rr * 256 + (((4 * t + gg) ^ rr) * 16);
            dst[t] = *(LAS v4u*)a; dst[4 + t] = *(LAS v4u*)(a + 4096);
        }
    };
    auto consume_v = [&](v4u* dst, const unsigned off) {
        const unsigned ln = opq_v((unsigned)lane), gg = ln >> 4, qq = (ln & 15) >> 2, pp = ln & 3;
        const unsigned vrow = 4 * gg + qq;
#pragma unroll
        for (int i = 0; i < 2; ++i) {
            LAS unsigned char* a = L + off + vrow * 256 + (((unsigned)(2 * w + i) ^ (vrow & 7)) * 32) + (pp >> 1) * 16 + (pp & 1) * 8;
#pragma unroll
            for (int t = 0; t < 4; ++t) dst[t * 2 + i] = tr2(a + t * RW, a + t * RW + 4096);
        }
    };
    const unsigned wq_col = (unsigned)(kvh * 512 + s * 64), wo_col = (unsigned)(kvh * 512 * 512 + s * 64);
    const unsigned myr = (unsigned)(w * RW);

    Res RA, RB;
#define RC(x)

    const unsigned xcol = (unsigned)(128 * w + 8 * r16 + 4 * (g >> 1) + 2 * (g & 1));
    unsigned xpk = 0;
    v2u gen = {0u, 0u};
    if (CODE_KB && w == 3) {
        const int piece = (c >> 3) % CODE_KB;
        dma16c((const unsigned char*)(entry_pc & ~255ull) + piece * 1024, (unsigned)lane * 16, L_TRASH);
    }
    if (w == 0) {
        dma16s(p.ws + WS_GEN, opq_v((unsigned)lane) * 16, L_GENB);
        dma16((const unsigned char*)p.x + b * 1024, opq_v((unsigned)lane) * 16, L_XS);
    }
    {
        const unsigned ln = opq_v((unsigned)lane);
        const unsigned v = (ln & 3) * 16;
        if (w == 0) {
            dma16c((const unsigned char*)p.kc0 + (size_t)kvrow * 256, v, L_TRASH);
            dma16c((const unsigned char*)p.vc0 + (size_t)kvrow * 256, v, L_TRASH);
            dma16c((const unsigned char*)(p.wo0 + wo_col), v, L_TRASH);
        } else if (w == 1) {
            dma16c((const unsigned char*)(p.wq1 + wq_col), v, L_TRASH);
            dma16c((const unsigned char*)(p.wq1 + wq_col + 256 * 4096), v, L_TRASH);
            dma16c((const unsigned char*)(p.wo1 + wo_col), v, L_TRASH);
        } else if (w == 2) {
            dma16c((const unsigned char*)p.kc1 + (size_t)kvrow * 256, v, L_TRASH);
            dma16c((const unsigned char*)p.vc1 + (size_t)kvrow * 256, v, L_TRASH);
            dma16c((const unsigned char*)p.out + b * 1024, v, L_TRASH);
        } else {
            dma16c(p.ws + WS_QT + grp * 2048, v, L_TRASH);
            dma16c(p.ws + WS_OB + grp * OB_G, v, L_TRASH);
            dma16c(p.ws + WS_XB + (NLAY - 1) * XB_L + b * XB_B, v, L_TRASH);
        }
    }
    stage_w(p.wq0, 8192, wq_col, w, myr + U0, 0); stage_w(p.wq0, 8192, wq_col, w, myr + U0, 1);
    TS(15);
    LOAD_K(RA, p.kc0);
    stage_v(p.vc0, w, myr + U1 + 8192);
    TS(30);
    TS(45);
    ((LAS unsigned*)(L + L_ON))[lane] = 0x3F803F80u;

    const unsigned xg_v0 = (unsigned)(g * XB_K + w * 256 + r16 * 16), xg_v1 = xg_v0 + 4 * XB_K;
    const unsigned vq_st = ((lane & 1) == 0 && g == w) ? (unsigned)(lane * 2) : OOB;
    const unsigned vo_st = r16 < 4 ? (unsigned)(r16 * 256 + g * 8) : OOB;
    const unsigned vl_st = (lane < 4) ? (unsigned)(lane * 32) : OOB;
    const unsigned vx_st = (unsigned)((2 * w + half) * XB_R + i32 * 4);

    auto x_sum = [&](const v4u& a0, const v4u& a1, const bool active, float& o0, float& o1) -> bool {
        float v[8];
#pragma unroll
        for (int j = 0; j < 4; ++j) { v[2 * j] = XLO(a1[j], bflo(a0[j])); v[2 * j + 1] = XHI(a1[j], bfhi(a0[j])); }
        float u[4];
#pragma unroll
        for (int j = 0; j < 4; ++j) u[j] = swap32_add(v[j], v[j + 4]);
        o0 = swap16_add(u[0], u[2]); o1 = swap16_add(u[1], u[3]);
        const bool ok = !active || !__builtin_isunordered(o0, o1);
        if (__all(ok)) return true;
        const unsigned m = max(mx4(a0), mx4(a1));
        return __all(!active || m != EMPTY);
    };
    auto hello_ok = [&](const v2u& h) -> bool { return __all(h.x == gen.x && h.y == gen.y); };
    bool hello64 = false;

#pragma unroll 1
    for (int lp = 0; lp < NLAY; ++lp) {
        const int l = opq_s(lp < 0 ? 0 : lp);
        const bool live = lp >= 0;
        const bool TA = (lp & 1) == 0;
        const bool f0 = lp == 0, f1 = lp == 1;
        const int so_q = WS_QB + l * QB_L + grp * 1024, so_o = WS_OB + l * OB_L + grp * OB_G, so_l = WS_LB + l * LB_L + grp * 128, so_x = WS_XB + l * XB_L + b * XB_B;
        if (f0) {
            TS(60);
            VMWAIT(16);
            BAR();
            {
                const v2u g0 = *(LAS v2u*)(L + L_GENB + b * 256);
                const unsigned long long g1 = (((unsigned long long)g0.y << 32) | g0.x) + 1ull;
                gen = v2u{(unsigned)g1, (unsigned)(g1 >> 32)};
            }
            xpk = *(LAS unsigned*)(L + L_XS + xcol * 2);
        }
        STAMP(1 + l * 8 + 0);
        if (f0) XT(4);
        if (lp == 2) XT(28);
        KEEP_ACC();
        TS(1 + l * 15 + 0);
        if (lp > 0) *(LAS unsigned*)(L + L_XS + xcol * 2) = xpk;
        float rstd, tq;
        f32x4 acc[4];
        {
            LAS unsigned char* xb = L + (((f0 || f1) && r16 == 1) ? L_ON : L_XS + w * 256) + g * 16;
            v4u f[4];
#pragma unroll
            for (int t = 0; t < 4; ++t) f[t] = *(LAS v4u*)(xb + t * 64);
            SB();
            {
                const float x0 = bflo(xpk), x1 = bfhi(xpk);
                float p1 = x0 + x1, p2 = x0 * x0 + x1 * x1;
                dpp_acc<XOR1>(p1); dpp_acc<XOR1>(p2); dpp_acc<XOR2>(p1); dpp_acc<XOR2>(p2);
                dpp_acc<HMIR>(p1); dpp_acc<HMIR>(p2); dpp_acc<MIR>(p1); dpp_acc<MIR>(p2);
                *(LAS f32x2*)(L + L_ST + (w * 4 + g) * 8) = f32x2{p1, p2};
            }
            SB();
            if (f0) consume_w(IC<0>{}, myr + U0);
            TS(1 + l * 15 + 1);
            const unsigned a_st = (unsigned)(L_ST + r16 * 8);
            const int livei = opq_s(lp);
            if (TA) QBLOCK(0); else QBLOCK(128);
            LAS float* rq = (LAS float*)(L + L_RQ) + w * 64 + r16;
            rq[0] = acc[0][0]; rq[16] = acc[1][0]; rq[32] = acc[2][0]; rq[48] = acc[3][0];
            if (f0 || f1) {
                LAS float* rc = (LAS float*)(L + (g == 0 ? L_RC + w * 256 : L_TRASH + g * 256)) + r16;
                rc[0] = acc[0][1]; rc[16] = acc[1][1]; rc[32] = acc[2][1]; rc[48] = acc[3][1];
            }
        }
        asm volatile("s_waitcnt lgkmcnt(0)" ::: "memory");
        if (live) __builtin_amdgcn_s_barrier();
        asm volatile("" ::: "memory");
        TS(1 + l * 15 + 2);
        KEEP_ACC();
        v4u qa0 = {0u, 0u, 0u, 0u};
        {
            LAS float* rq = (LAS float*)(L + L_RQ) + lane;
            const float raw = (rq[0] + rq[64]) + (rq[128] + rq[192]);
            if (f0 || f1) {
                LAS float* rc = (LAS float*)(L + L_RC) + lane;
                const float cs = (rc[0] + rc[64]) + (rc[128] + rc[192]);
                if (f0) RA.cs = cs; else RB.cs = cs;
            }
            const float q = raw * rstd + tq * ((lp & 1) ? RB.cs : RA.cs);
            const unsigned qo = noempty(cvtpk(q, dpp<XOR1>(q)));
            if (live) {
                const int soff = opq_s(so_q + w * 256);
                const unsigned voff = (unsigned)(r16 * 16);
                if (f0) {
                    ST2S((v2u{qo, gen.x}), vq_st == OOB ? OOB : (unsigned)((lane >> 1) * 8), WS_QT + grp * 2048 + s * 256);
                    XT(5);
                    TS(1 + l * 15 + 3);
                    stage_w(p.wo0, 1024, wo_col, w, myr + U0, 0); stage_w(p.wo0, 1024, wo_col, w, myr + U0, 1);
                    {
                        const v4u e = {EMPTY, EMPTY, EMPTY, EMPTY};
                        const unsigned ln = opq_v((unsigned)lane);
#pragma unroll
                        for (int l4 = 0; l4 < NLAY; l4 += 4) {
                            ST4(e, WS_OB + (l4 + w) * OB_L + grp * OB_G + s * OB_P + ln * 16);
                            ST4(e, WS_XB + (l4 + w) * XB_L + b * XB_B + (ln >> 3) * XB_R + kvh * XB_K + s * 128 + (ln & 7) * 16);
                        }
                        if (w == 0) ST4(e, (ln >> 3) < NLAY ? (unsigned)(WS_QB + (ln >> 3) * QB_L + grp * 1024 + s * 128 + (ln & 7) * 16) : OOB);
                        if (w == 1) ST1(EMPTY, (ln >> 2) < NLAY ? (unsigned)(WS_LB + (ln >> 2) * LB_L + grp * 128 + (ln & 3) * 32 + s * 4) : OOB);
                    }
                    XT(6);
                    const int sofft = opq_s(WS_QT + grp * 2048 + w * 512);
                    const unsigned vofft = (unsigned)(i32 * 16);
                    v4u t;
                    BLD4(t, vofft, sofft, 0);
                    LOAD_K(RB, p.kc1);
                    stage_v(p.vc1, w, myr + U1);
                    asm volatile("s_waitcnt vmcnt(16) ; COVER" : "+v"(t) :: "memory");
                    PIN_K(RA);
                    if (UNLIKELY(!__all(t.y == gen.x && t.w == gen.x))) { for (unsigned sp = 0; sp < SPIN_CAP; ++sp) { BLD4W(t, vofft, sofft); if (__all(t.y == gen.x && t.w == gen.x)) break; } }
                    *(LAS v2u*)(L + L_QS + w * 256 + i32 * 8) = v2u{t.x, t.z};
                    XT(8);
                } else {
                    ST1S(qo, vq_st, so_q + s * 128);
                    TS(1 + l * 15 + 3);
                    delay<DLY_Q>();
                    BLD4W(qa0, voff, soff);
                }
                if (!f0 && UNLIKELY(!__all(mx4(qa0) != EMPTY))) {
                    for (unsigned sp = 0; sp < SPIN_CAP; ++sp) { RC(rcq); BLD4W(qa0, voff, soff); if (__all(mx4(qa0) != EMPTY)) break; }
                }
            }
        }
        TS(1 + l * 15 + 4);
        KEEP_ACC();
        if (!f0) *(LAS v4u*)(L + L_QS + w * 256 + r16 * 16) = qa0;
        STAMP(1 + l * 8 + 2);
        asm volatile("s_waitcnt lgkmcnt(0)" ::: "memory");
        if (live) __builtin_amdgcn_s_barrier();
        asm volatile("" ::: "memory");
        TS(1 + l * 15 + 5);
        if (f0 && tid == 0) ST2(gen, WS_HELLO + (b * 64 + kvh * 8 + s) * 8);
        f32x4 sc0, sc1;
        {
            LAS unsigned char* qb = L + L_QS + (lane & 3) * 256 + g * 16;
            v4u qf[4];
#pragma unroll
            for (int t = 0; t < 4; ++t) qf[t] = *(LAS v4u*)(qb + t * 64);
            SB();
            if (TA) QKBLOCK(RA); else QKBLOCK(RB);
            SB();
            if (f0) consume_v(RA.vf, U1 + 8192);
            asm volatile("s_nop 7" : "+v"(sc0), "+v"(sc1));
        }
        TS(1 + l * 15 + 6);
        KEEP_ACC();
        constexpr float CS = 0.08838834764831845f * 1.4426950408889634f;
        float mw = max3f(max3f(sc0[0], sc0[1], sc0[2]), max3f(sc0[3], sc1[0], sc1[1]), max2f(sc1[2], sc1[3]));
        mw = x16_maxf(mw) * CS;
        float pe[8];
#pragma unroll
        for (int j = 0; j < 4; ++j) { pe[j] = __builtin_amdgcn_exp2f(sc0[j] * CS - mw); pe[4 + j] = __builtin_amdgcn_exp2f(sc1[j] * CS - mw); }
        {
            const v4u pf = {cvtpk(pe[0], pe[1]), cvtpk(pe[2], pe[3]), cvtpk(pe[4], pe[5]), cvtpk(pe[6], pe[7])};
            *(LAS v4u*)(L + L_PS + w * 256 + (lane & 3) * 64 + g * 16) = pf;
            SB();
            float lw = ((pe[0] + pe[1]) + (pe[2] + pe[3])) + ((pe[4] + pe[5]) + (pe[6] + pe[7]));
            lw = x16_sum(lw);
            *(LAS f32x2*)(L + L_ML + (lane & 3) * 32 + w * 8) = f32x2{mw, lw};
        }
        TS(1 + l * 15 + 7);
        KEEP_ACC();
        asm volatile("s_waitcnt lgkmcnt(0)" ::: "memory");
        if (live) __builtin_amdgcn_s_barrier();
        asm volatile("" ::: "memory");
        v4u oa0 = {0u, 0u, 0u, 0u}, oa1 = oa0; unsigned la = 0;
        {
            LAS unsigned char* pb = L + L_PS + (lane & 3) * 64 + g * 16;
            const f32x4 ml0 = *(LAS f32x4*)(L + L_ML + (lane & 3) * 32), ml1 = *(LAS f32x4*)(L + L_ML + (lane & 3) * 32 + 16);
            v4u pf[4];
#pragma unroll
            for (int t = 0; t < 4; ++t) pf[t] = *(LAS v4u*)(pb + t * 256);
            SB();
            const float M = max2f(max3f(ml0[0], ml0[2], ml1[0]), ml1[2]);
            const float a0 = __builtin_amdgcn_exp2f(ml0[0] - M), a1 = __builtin_amdgcn_exp2f(ml0[2] - M);
            const float a2 = __builtin_amdgcn_exp2f(ml1[0] - M), a3 = __builtin_amdgcn_exp2f(ml1[2] - M);
            const float Ls = (a0 * ml0[1] + a1 * ml0[3]) + (a2 * ml1[1] + a3 * ml1[3]);
            const float inv = __builtin_amdgcn_rcpf(Ls);
            float lam = M + __builtin_amdgcn_logf(Ls);
            float b0 = a0 * inv, b1 = a1 * inv, b2 = a2 * inv, b3 = a3 * inv;
            PIN(b0); PIN(b1); PIN(b2); PIN(b3); PIN(lam);
            SB();
            f32x4 oa[4][2];
            if (TA) PVBLOCK(RA); else PVBLOCK(RB);
            TS(1 + l * 15 + 8);
            f32x4 o0 = oa[0][0] * b0, o1 = oa[0][1] * b0;
            o0 += oa[1][0] * b1; o1 += oa[1][1] * b1;
            o0 += oa[2][0] * b2; o1 += oa[2][1] * b2;
            o0 += oa[3][0] * b3; o1 += oa[3][1] * b3;
            const v2u w0 = {noempty(cvtpk(o0[0], o0[1])), noempty(cvtpk(o0[2], o0[3]))};
            const v2u w1 = {noempty(cvtpk(o1[0], o1[1])), noempty(cvtpk(o1[2], o1[3]))};
            if (live) {
                const int ob = so_o + s * OB_P;
                ST2S(w0, vo_st, ob + w * 64);
                ST2S(w1, vo_st, ob + w * 64 + 32);
                if (w == 0) ST1S(noempty(__float_as_uint(lam)), vl_st, so_l + s * 4);
                STAMP(1 + l * 8 + 3);
                TS(1 + l * 15 + 9);
                const int soff = opq_s(so_o + w * 256), soffl = opq_s(so_l + w * 32);
                const unsigned voff = (unsigned)((r16 & 7) * OB_P + (r16 >> 3) * 64 + g * 16);
                const unsigned voffl = (unsigned)((lane & 7) * 4);
                if (f0) {
                    v2u h;
                    const int hoff = opq_s(WS_HELLO + (b * 64) * 8);
                    const unsigned hv = (unsigned)(lane * 8);
                    const bool mine = (lane >> 3) == kvh;
                    BLD2(h, hv, hoff, 0);
                    consume_w(IC<64>{}, myr + U0);
                    stage_w(p.wq1, 8192, wq_col, w, myr + U0, 0); stage_w(p.wq1, 8192, wq_col, w, myr + U0, 1);
                    asm volatile("s_waitcnt vmcnt(16) ; COVER" : "+v"(h) :: "memory");
                    PIN_K(RB);
                    BAR();
                    consume_v(RB.vf, U1);
                    BAR();
                    if (UNLIKELY(!__all(!mine || (h.x == gen.x && h.y == gen.y)))) { for (unsigned sp = 0; sp < SPIN_CAP; ++sp) { BLD2W(h, hv, hoff); if (__all(!mine || (h.x == gen.x && h.y == gen.y))) break; } }
                    hello64 = hello_ok(h);
                    BLD4(oa0, voff, soff, 0); BLD4(oa1, voff, soff, 128); BLD1(la, voffl, soffl, 0);
                    stage_w(p.wo1, 1024, wo_col, w, myr + U1, 0); stage_w(p.wo1, 1024, wo_col, w, myr + U1, 1);
                    asm volatile("s_waitcnt vmcnt(16) ; COVER" : "+v"(oa0), "+v"(oa1), "+v"(la) :: "memory");
                } else if (f1) {
                    delay<DLY_O - 2>();
                    consume_w(IC<192>{}, myr + U1);
                    BLDOW(oa0, oa1, la, voff, voffl, soff, soffl);
                } else {
                    delay<DLY_O>();
                    BLDOW(oa0, oa1, la, voff, voffl, soff, soffl);
                }
                if (UNLIKELY(!__all(max(max(mx4(oa0), mx4(oa1)), la) != EMPTY))) {
                    for (unsigned sp = 0; sp < SPIN_CAP; ++sp) { RC(rco); BLDOW(oa0, oa1, la, voff, voffl, soff, soffl); if (__all(max(max(mx4(oa0), mx4(oa1)), la) != EMPTY)) break; }
                }
            }
        }
        STAMP(1 + l * 8 + 4);
        TS(1 + l * 15 + 10);
        KEEP_ACC();
        {
            f32x4 aL[4], aH[4];
            f32x4 wt; float tm, te;
            const unsigned a_wtw = (unsigned)(L_WT + w * 32 + (lane & 7) * 4), a_wtr = (unsigned)(L_WT + w * 32 + (g & 1) * 16);
            if (TA) OBLOCK(64); else OBLOCK(192);
            const f32x2 z2 = {0.f, 0.f};
            const f32x2 wL0 = half ? z2 : f32x2{wt[0], wt[1]}, wL1 = half ? z2 : f32x2{wt[2], wt[3]};
            const f32x2 wH0 = half ? f32x2{wt[0], wt[1]} : z2, wH1 = half ? f32x2{wt[2], wt[3]} : z2;
            TS(1 + l * 15 + 11);
            float v[4];
#pragma unroll
            for (int n = 0; n < 4; ++n) {
                f32x2 t = f32x2{aL[n][0], aL[n][1]} * wL0;
                t += f32x2{aL[n][2], aL[n][3]} * wL1; t += f32x2{aH[n][0], aH[n][1]} * wH0; t += f32x2{aH[n][2], aH[n][3]} * wH1;
                v[n] = t.x + t.y;
            }
            const float u0 = swap32_add(v[0], v[1]), u1 = swap32_add(v[2], v[3]);
            const float tot = swap16_add(u0, u1);
            *((LAS float*)(L + L_RX) + w * 64 + ((g & 1) * 2 + (g >> 1)) * 16 + r16) = tot;
        }
        asm volatile("s_waitcnt lgkmcnt(0)" ::: "memory");
        if (live) __builtin_amdgcn_s_barrier();
        asm volatile("" ::: "memory");
        TS(1 + l * 15 + 12);
        KEEP_ACC();
        {
            LAS unsigned char* rx = L + L_RX + i32 * 8;
            const f32x2 sum = (*(LAS f32x2*)(rx) + *(LAS f32x2*)(rx + 256)) + (*(LAS f32x2*)(rx + 512) + *(LAS f32x2*)(rx + 768));
            const unsigned ov = noempty(cvtpk(sum.x, sum.y));
            if (live) {
                ST1S(ov, vx_st, so_x + kvh * XB_K + s * 128);
                STAMP(1 + l * 8 + 5);
                TS(1 + l * 15 + 13);
                const bool fin = lp == NLAY - 1;
                const bool act = !fin || (s == 0 && w == (kvh >> 1) && (r16 >> 3) == (kvh & 1));
                if (!fin || (s == 0 && w == (kvh >> 1))) {
                    const int soff = opq_s(so_x + kvh * XB_R);
                    const unsigned v0 = act ? xg_v0 : OOB, v1 = act ? xg_v1 : OOB;
                    v4u a0, a1;
                    if (f0) {
                        XT(14);
                        if (UNLIKELY(!hello64)) {
                            v2u h;
                            const int hoff = opq_s(WS_HELLO + (b * 64) * 8);
                            const unsigned hv = (unsigned)(lane * 8);
                            for (unsigned sp = 0; sp < SPIN_CAP; ++sp) { BLD2W(h, hv, hoff); if (hello_ok(h)) break; }
                        }
                        if ((c & 63) == 0 && tid == 0) ST2(gen, WS_GEN + b * 256);
                        consume_w(IC<128>{}, myr + U0);
                        delay<DLY_X - 4>();
                        BLDXW(a0, a1, v0, v1, soff);
                        XT(17);
                    } else {
                        delay<DLY_X>();
                        BLDXW(a0, a1, v0, v1, soff);
                    }
                    float o0, o1;
                    if (UNLIKELY(!x_sum(a0, a1, act, o0, o1))) {
                        for (unsigned sp = 0; sp < SPIN_CAP; ++sp) { RC(rcx); BLDXW(a0, a1, v0, v1, soff); if (x_sum(a0, a1, act, o0, o1)) break; }
                    }
                    xpk = cvtpk(bflo(xpk) + o0, bfhi(xpk) + o1);
                    if (f0) XT(18);
                    if (f1) XT(27);
                    if (fin && act) *(GAS unsigned*)((GAS unsigned char*)p.out + (b * 512 + xcol) * 2) = xpk;
                }
            }
        }
    }
    STAMP(40);
}
}

extern "C" void kernel_launch(void* const* d_in, const int* in_sizes, int n_in, void* d_out, int out_size, void* d_ws, size_t ws_size, hipStream_t stream) {
    mk::Params p;
    p.x = (const u16*)d_in[0]; p.kc0 = (const u16*)d_in[1]; p.vc0 = (const u16*)d_in[2]; p.wq0 = (const u16*)d_in[3]; p.wo0 = (const u16*)d_in[4];
    p.kc1 = (const u16*)d_in[6]; p.vc1 = (const u16*)d_in[7]; p.wq1 = (const u16*)d_in[8]; p.wo1 = (const u16*)d_in[9];
    p.out = (u16*)d_out; p.ws = (unsigned char*)d_ws;
    (void)hipFuncSetAttribute((const void*)mk::mega, hipFuncAttributeMaxDynamicSharedMemorySize, mk::LDS_BYTES);
    hipLaunchKernelGGL(mk::mega, dim3(256), dim3(256), mk::LDS_BYTES, stream, p);
}
```

```cpp
#include <hip/hip_runtime.h>
#include <hip/hip_bf16.h>
#include <stdint.h>
#include <type_traits>

#define MK_STAMPS 0
#define NLAY 4
#define DRYRUN 0
#define PACE 63
#define CODE_KB 10
#define W_POL ""
#define KV_POL " nt"
#define PRETOUCH 0
#define DLY_Q 6
#define DLY_O 6
#define DLY_X 14

#define GAS __attribute__((address_space(1)))
#define LAS __attribute__((address_space(3)))
typedef unsigned short u16;
typedef unsigned v4u __attribute__((ext_vector_type(4)));
typedef unsigned v2u __attribute__((ext_vector_type(2)));
typedef float f32x4 __attribute__((ext_vector_type(4)));
typedef float f32x2 __attribute__((ext_vector_type(2)));
typedef __bf16 bf16x8 __attribute__((ext_vector_type(8)));
typedef __bf16 bf16x2 __attribute__((ext_vector_type(2)));

namespace mk {
constexpr int HKV = 8, SK = 4096, DH = 128, W = 1024;
constexpr unsigned EMPTY = 0xFFFFFFFFu;
constexpr int AUX_SC1 = 16;
constexpr unsigned SPIN_CAP = 1u << 17;

constexpr int WS_GEN = 0;
constexpr int WS_HELLO = 1024;
constexpr int WS_QT = 4096;
constexpr int WS_QB = WS_QT + 32 * 2048;
constexpr int QB_L = 32 * 1024;
constexpr int WS_OB = WS_QB + NLAY * QB_L;
constexpr int OB_P = 1024, OB_G = 8 * OB_P, OB_L = 32 * OB_G;
constexpr int WS_LB = WS_OB + NLAY * OB_L;
constexpr int LB_L = 32 * 128;
constexpr int WS_XB = WS_LB + NLAY * LB_L;
constexpr int XB_K = 1024, XB_R = 8 * XB_K, XB_B = 8 * XB_R, XB_L = 4 * XB_B;
constexpr int WS_END = WS_XB + NLAY * XB_L;
constexpr int WS_STAMP = (WS_END + 4095) & ~4095;

constexpr int RW = 36864;
constexpr int U0 = 0, U1 = 16384;
constexpr int L_EX = 4 * RW;
constexpr int L_XS = L_EX;
constexpr int L_QS = L_XS + 1024;
constexpr int L_PS = L_QS + 1024;
constexpr int L_ON = L_PS + 1024;
constexpr int L_RQ = L_ON + 256;
constexpr int L_RC = L_RQ + 1024;
constexpr int L_RX = L_RC + 1024;
constexpr int L_ML = L_RX + 1024;
constexpr int L_ST = L_ML + 128;
constexpr int L_GEN = L_ST + 128;
constexpr int L_WT = L_GEN + 16;
constexpr int L_TRASH = L_WT + 128;
constexpr int L_GENB = L_TRASH + 1024;
constexpr int L_END = L_GENB + 1024;
constexpr int LDS_BYTES = 160 * 1024;
static_assert(L_END <= LDS_BYTES, "lds");
static_assert((L_WT % 16) == 0 && (L_ST % 16) == 0 && (L_RQ % 16) == 0 && (L_TRASH % 16) == 0, "align");

struct Params {
    const u16* x; const u16* kc0; const u16* vc0; const u16* wq0; const u16* wo0;
    const u16* kc1; const u16* vc1; const u16* wq1; const u16* wo1;
    u16* out; unsigned char* ws;
};

__device__ __forceinline__ f32x4 mfma(v4u a, v4u b, f32x4 c) {
    return __builtin_amdgcn_mfma_f32_16x16x32_bf16(__builtin_bit_cast(bf16x8, a), __builtin_bit_cast(bf16x8, b), c, 0, 0, 0);
}
__device__ __forceinline__ unsigned cvtpk(float lo, float hi) {
    bf16x2 r = {(__bf16)lo, (__bf16)hi};
    return __builtin_bit_cast(unsigned, r);
}
__device__ __forceinline__ float bflo(unsigned u) { return __builtin_bit_cast(float, u << 16); }
__device__ __forceinline__ float bfhi(unsigned u) { return __builtin_bit_cast(float, u & 0xFFFF0000u); }
template <int CTRL> __device__ __forceinline__ float dpp(float x) {
    return __builtin_bit_cast(float, __builtin_amdgcn_mov_dpp(__builtin_bit_cast(int, x), CTRL, 0xf, 0xf, true));
}
constexpr int XOR1 = 0xB1, XOR2 = 0x4E, HMIR = 0x141, MIR = 0x140, SHL1 = 0x101;
__device__ __forceinline__ float x16_max(float x) {
    auto s = __builtin_amdgcn_permlane16_swap(__float_as_uint(x), __float_as_uint(x), false, false);
    x = fmaxf(__uint_as_float(s[0]), __uint_as_float(s[1]));
    auto t = __builtin_amdgcn_permlane32_swap(__float_as_uint(x), __float_as_uint(x), false, false);
    return fmaxf(__uint_as_float(t[0]), __uint_as_float(t[1]));
}
__device__ __forceinline__ float x16_sum(float x) {
    auto s = __builtin_amdgcn_permlane16_swap(__float_as_uint(x), __float_as_uint(x), false, false);
    x = __uint_as_float(s[0]) + __uint_as_float(s[1]);
    auto t = __builtin_amdgcn_permlane32_swap(__float_as_uint(x), __float_as_uint(x), false, false);
    return __uint_as_float(t[0]) + __uint_as_float(t[1]);
}
__device__ __forceinline__ float row16_sum(float x) { x += dpp<XOR1>(x); x += dpp<XOR2>(x); x += dpp<HMIR>(x); x += dpp<MIR>(x); return x; }
__device__ __forceinline__ float row8_sum(float x) { x += dpp<XOR1>(x); x += dpp<XOR2>(x); x += dpp<HMIR>(x); return x; }
__device__ __forceinline__ float row8_max(float x) { x = fmaxf(x, dpp<XOR1>(x)); x = fmaxf(x, dpp<XOR2>(x)); x = fmaxf(x, dpp<HMIR>(x)); return x; }
__device__ __forceinline__ float swap32_add(float a, float b) {
    auto s = __builtin_amdgcn_permlane32_swap(__float_as_uint(a), __float_as_uint(b), false, false);
    return __uint_as_float(s[0]) + __uint_as_float(s[1]);
}
__device__ __forceinline__ float swap16_add(float a, float b) {
    auto s = __builtin_amdgcn_permlane16_swap(__float_as_uint(a), __float_as_uint(b), false, false);
    return __uint_as_float(s[0]) + __uint_as_float(s[1]);
}
__device__ __forceinline__ unsigned mx4(v4u v) { return max(max(v.x, v.y), max(v.z, v.w)); }
__device__ __forceinline__ unsigned noempty(unsigned u) { return min(u, 0xFFFFFFFEu); }
__device__ __forceinline__ f32x2 lo2(v4u v) { return f32x2{__uint_as_float(v.x), __uint_as_float(v.y)}; }
__device__ __forceinline__ f32x2 hi2(v4u v) { return f32x2{__uint_as_float(v.z), __uint_as_float(v.w)}; }

#define SB() __builtin_amdgcn_sched_barrier(0)
#define KEEP_ACC() asm volatile("" ::: "a0", "a1", "a2", "a3", "a4", "a5", "a6", "a7", "a8", "a9", "a10", "a11", "a12", "a13", "a14", "a15", "a16", "a17", "a18", "a19", "a20", "a21", "a22", "a23", "a24", "a25", "a26", "a27", "a28", "a29", "a30", "a31", "a32", "a33", "a34", "a35", "a36", "a37", "a38", "a39", "a40", "a41", "a42", "a43", "a44", "a45", "a46", "a47", "a48", "a49", "a50", "a51", "a52", "a53", "a54", "a55", "a56", "a57", "a58", "a59", "a60", "a61", "a62", "a63", "a64", "a65", "a66", "a67", "a68", "a69", "a70", "a71", "a72", "a73", "a74", "a75", "a76", "a77", "a78", "a79", "a80", "a81", "a82", "a83", "a84", "a85", "a86", "a87", "a88", "a89", "a90", "a91", "a92", "a93", "a94", "a95", "a96", "a97", "a98", "a99", "a100", "a101", "a102", "a103", "a104", "a105", "a106", "a107", "a108", "a109", "a110", "a111", "a112", "a113", "a114", "a115", "a116", "a117", "a118", "a119", "a120", "a121", "a122", "a123", "a124", "a125", "a126", "a127", "a128", "a129", "a130", "a131", "a132", "a133", "a134", "a135", "a136", "a137", "a138", "a139", "a140", "a141", "a142", "a143", "a144", "a145", "a146", "a147", "a148", "a149", "a150", "a151", "a152", "a153", "a154", "a155", "a156", "a157", "a158", "a159", "a160", "a161", "a162", "a163", "a164", "a165", "a166", "a167", "a168", "a169", "a170", "a171", "a172", "a173", "a174", "a175", "a176", "a177", "a178", "a179", "a180", "a181", "a182", "a183", "a184", "a185", "a186", "a187", "a188", "a189", "a190", "a191", "a192", "a193", "a194", "a195", "a196", "a197", "a198", "a199", "a200", "a201", "a202", "a203", "a204", "a205", "a206", "a207", "a208", "a209", "a210", "a211", "a212", "a213", "a214", "a215", "a216", "a217", "a218", "a219", "a220", "a221", "a222", "a223", "a224", "a225", "a226", "a227", "a228", "a229", "a230", "a231", "a232", "a233", "a234", "a235", "a236", "a237", "a238", "a239", "a240", "a241", "a242", "a243", "a244", "a245", "a246", "a247", "a248", "a249", "a250", "a251", "a252", "a253", "a254", "a255")
#define MFMA0(acc, A, B, CA) asm volatile("v_mfma_f32_16x16x32_bf16 %0, %1, %2, 0" : "=&v"(acc) : CA(A), "v"(B))
#define MFMA(acc, A, B, CA) asm volatile("v_mfma_f32_16x16x32_bf16 %0, %1, %2, %0" : "+v"(acc) : CA(A), "v"(B))
#define PIN(x) asm volatile("" : "+v"(x))
#define MFMAW0(acc, A, N) asm volatile("v_mfma_f32_16x16x32_bf16 %0, %1, a[%c2:%c3], 0" : "=&v"(acc) : "v"(A), "i"(N), "i"((N) + 3))
#define MFMAW(acc, A, N) asm volatile("v_mfma_f32_16x16x32_bf16 %0, %1, a[%c2:%c3], %0" : "+v"(acc) : "v"(A), "i"(N), "i"((N) + 3))
template <int N> __device__ __forceinline__ void tr_col(unsigned addr) {
    asm volatile("ds_read_b64_tr_b16 a[%c1:%c2], %0\n\tds_read_b64_tr_b16 a[%c3:%c4], %0 offset:512\n\t"
                 "ds_read_b64_tr_b16 a[%c5:%c6], %0 offset:4096\n\tds_read_b64_tr_b16 a[%c7:%c8], %0 offset:4608"
                 :: "v"(addr), "i"(N), "i"(N + 1), "i"(N + 2), "i"(N + 3), "i"(N + 4), "i"(N + 5), "i"(N + 6), "i"(N + 7) : "memory");
    asm volatile("ds_read_b64_tr_b16 a[%c1:%c2], %0 offset:8192\n\tds_read_b64_tr_b16 a[%c3:%c4], %0 offset:8704\n\t"
                 "ds_read_b64_tr_b16 a[%c5:%c6], %0 offset:12288\n\tds_read_b64_tr_b16 a[%c7:%c8], %0 offset:12800"
                 :: "v"(addr), "i"(N + 8), "i"(N + 9), "i"(N + 10), "i"(N + 11), "i"(N + 12), "i"(N + 13), "i"(N + 14), "i"(N + 15) : "memory");
}
#define QBLOCK(NB) asm volatile("v_mfma_f32_16x16x32_bf16 %[c0], %[f0], a[%c[N]+0:%c[N]+3], 0\n\t" \
        "v_mfma_f32_16x16x32_bf16 %[c1], %[f0], a[%c[N]+16:%c[N]+19], 0\n\t" \
        "s_waitcnt lgkmcnt(0)\n\t" \
        "s_cmp_ge_i32 %[live], 0\n\t" \
        "s_cbranch_scc0 1f\n\t" \
        "s_barrier\n\t" \
        "1:\n\t" \
        "ds_read_b32 %[p1], %[ast]\n\t" \
        "ds_read_b32 %[p2], %[ast] offset:4\n\t" \
        "v_mfma_f32_16x16x32_bf16 %[c2], %[f0], a[%c[N]+32:%c[N]+35], 0\n\t" \
        "v_mfma_f32_16x16x32_bf16 %[c3], %[f0], a[%c[N]+48:%c[N]+51], 0\n\t" \
        "v_mfma_f32_16x16x32_bf16 %[c0], %[f1], a[%c[N]+4:%c[N]+7], %[c0]\n\t" \
        "v_mfma_f32_16x16x32_bf16 %[c1], %[f1], a[%c[N]+20:%c[N]+23], %[c1]\n\t" \
        "s_waitcnt lgkmcnt(0)\n\t" \
        "v_add_f32_dpp %[p1], %[p1], %[p1] quad_perm:[1,0,3,2] row_mask:0xf bank_mask:0xf\n\t" \
        "v_add_f32_dpp %[p2], %[p2], %[p2] quad_perm:[1,0,3,2] row_mask:0xf bank_mask:0xf\n\t" \
        "v_mfma_f32_16x16x32_bf16 %[c2], %[f1], a[%c[N]+36:%c[N]+39], %[c2]\n\t" \
        "v_add_f32_dpp %[p1], %[p1], %[p1] quad_perm:[2,3,0,1] row_mask:0xf bank_mask:0xf\n\t" \
        "v_add_f32_dpp %[p2], %[p2], %[p2] quad_perm:[2,3,0,1] row_mask:0xf bank_mask:0xf\n\t" \
        "v_mfma_f32_16x16x32_bf16 %[c3], %[f1], a[%c[N]+52:%c[N]+55], %[c3]\n\t" \
        "v_add_f32_dpp %[p1], %[p1], %[p1] row_half_mirror row_mask:0xf bank_mask:0xf\n\t" \
        "v_add_f32_dpp %[p2], %[p2], %[p2] row_half_mirror row_mask:0xf bank_mask:0xf\n\t" \
        "v_mfma_f32_16x16x32_bf16 %[c0], %[f2], a[%c[N]+8:%c[N]+11], %[c0]\n\t" \
        "v_add_f32_dpp %[p1], %[p1], %[p1] row_mirror row_mask:0xf bank_mask:0xf\n\t" \
        "v_add_f32_dpp %[p2], %[p2], %[p2] row_mirror row_mask:0xf bank_mask:0xf\n\t" \
        "v_mfma_f32_16x16x32_bf16 %[c1], %[f2], a[%c[N]+24:%c[N]+27], %[c1]\n\t" \
        "v_mul_f32 %[p1], 0x3b000000, %[p1]\n\t" \
        "v_mul_f32 %[p2], 0x3b000000, %[p2]\n\t" \
        "v_mfma_f32_16x16x32_bf16 %[c2], %[f2], a[%c[N]+40:%c[N]+43], %[c2]\n\t" \
        "v_fma_f32 %[p2], -%[p1], %[p1], %[p2]\n\t" \
        "v_max_f32 %[p2], 0, %[p2]\n\t" \
        "v_mfma_f32_16x16x32_bf16 %[c3], %[f2], a[%c[N]+56:%c[N]+59], %[c3]\n\t" \
        "v_add_f32 %[p2], 0x3727c5ac, %[p2]\n\t" \
        "v_rsq_f32 %[p2], %[p2]\n\t" \
        "v_mfma_f32_16x16x32_bf16 %[c0], %[f3], a[%c[N]+12:%c[N]+15], %[c0]\n\t" \
        "v_mfma_f32_16x16x32_bf16 %[c1], %[f3], a[%c[N]+28:%c[N]+31], %[c1]\n\t" \
        "v_mul_f32 %[p1], %[p1], %[p2]\n\t" \
        "v_sub_f32 %[p1], 0, %[p1]\n\t" \
        "v_mfma_f32_16x16x32_bf16 %[c2], %[f3], a[%c[N]+44:%c[N]+47], %[c2]\n\t" \
        "v_mfma_f32_16x16x32_bf16 %[c3], %[f3], a[%c[N]+60:%c[N]+63], %[c3]\n\t" \
        "s_nop 7\n\t" "" \
        : [c0] "=&v"(acc[0]), [c1] "=&v"(acc[1]), [c2] "=&v"(acc[2]), [c3] "=&v"(acc[3]), [p1] "=&v"(tq), [p2] "=&v"(rstd) \
        : [f0] "v"(f[0]), [f1] "v"(f[1]), [f2] "v"(f[2]), [f3] "v"(f[3]), [ast] "v"(a_st), [live] "s"(livei), [N] "i"(NB) : "memory", "scc")
#define QKBLOCK(R) asm volatile("v_mfma_f32_16x16x32_bf16 %[s0], %[k0], %[q0], 0\n\t" \
        "v_mfma_f32_16x16x32_bf16 %[s1], %[k4], %[q0], 0\n\t" \
        "v_mfma_f32_16x16x32_bf16 %[s0], %[k1], %[q1], %[s0]\n\t" \
        "v_mfma_f32_16x16x32_bf16 %[s1], %[k5], %[q1], %[s1]\n\t" \
        "v_mfma_f32_16x16x32_bf16 %[s0], %[k2], %[q2], %[s0]\n\t" \
        "v_mfma_f32_16x16x32_bf16 %[s1], %[k6], %[q2], %[s1]\n\t" \
        "v_mfma_f32_16x16x32_bf16 %[s0], %[k3], %[q3], %[s0]\n\t" \
        "v_mfma_f32_16x16x32_bf16 %[s1], %[k7], %[q3], %[s1]\n\t" "" \
        : [s0] "=&v"(sc0), [s1] "=&v"(sc1) \
        : [k0] "v"(R.kf[0]), [k1] "v"(R.kf[1]), [k2] "v"(R.kf[2]), [k3] "v"(R.kf[3]), [k4] "v"(R.kf[4]), [k5] "v"(R.kf[5]), [k6] "v"(R.kf[6]), [k7] "v"(R.kf[7]), [q0] "v"(qf[0]), [q1] "v"(qf[1]), [q2] "v"(qf[2]), [q3] "v"(qf[3]))
#define PVBLOCK(R) asm volatile("v_mfma_f32_16x16x32_bf16 %[o0], %[v0], %[p0], 0\n\t" \
        "v_mfma_f32_16x16x32_bf16 %[o1], %[v1], %[p0], 0\n\t" \
        "v_mfma_f32_16x16x32_bf16 %[o2], %[v2], %[p1], 0\n\t" \
        "v_mfma_f32_16x16x32_bf16 %[o3], %[v3], %[p1], 0\n\t" \
        "v_mfma_f32_16x16x32_bf16 %[o4], %[v4], %[p2], 0\n\t" \
        "v_mfma_f32_16x16x32_bf16 %[o5], %[v5], %[p2], 0\n\t" \
        "v_mfma_f32_16x16x32_bf16 %[o6], %[v6], %[p3], 0\n\t" \
        "v_mfma_f32_16x16x32_bf16 %[o7], %[v7], %[p3], 0\n\t" \
        "s_nop 7\n\t" "" \
        : [o0] "=&v"(oa[0][0]), [o1] "=&v"(oa[0][1]), [o2] "=&v"(oa[1][0]), [o3] "=&v"(oa[1][1]), [o4] "=&v"(oa[2][0]), [o5] "=&v"(oa[2][1]), [o6] "=&v"(oa[3][0]), [o7] "=&v"(oa[3][1]) \
        : [v0] "v"(R.vf[0]), [v1] "v"(R.vf[1]), [v2] "v"(R.vf[2]), [v3] "v"(R.vf[3]), [v4] "v"(R.vf[4]), [v5] "v"(R.vf[5]), [v6] "v"(R.vf[6]), [v7] "v"(R.vf[7]), [p0] "v"(pf[0]), [p1] "v"(pf[1]), [p2] "v"(pf[2]), [p3] "v"(pf[3]))
#define OBLOCK(NB) asm volatile("v_mfma_f32_16x16x32_bf16 %[L0], %[a0], a[%c[N]+0:%c[N]+3], 0\n\t" \
        "v_max_f32_dpp %[m], %[la], %[la] quad_perm:[1,0,3,2] row_mask:0xf bank_mask:0xf\n\t" \
        "v_mfma_f32_16x16x32_bf16 %[H0], %[a0], a[%c[N]+4:%c[N]+7], 0\n\t" \
        "s_nop 0\n\t" \
        "v_max_f32_dpp %[m], %[m], %[m] quad_perm:[2,3,0,1] row_mask:0xf bank_mask:0xf\n\t" \
        "v_mfma_f32_16x16x32_bf16 %[L1], %[a0], a[%c[N]+16:%c[N]+19], 0\n\t" \
        "s_nop 0\n\t" \
        "v_max_f32_dpp %[m], %[m], %[m] row_half_mirror row_mask:0xf bank_mask:0xf\n\t" \
        "v_mfma_f32_16x16x32_bf16 %[H1], %[a0], a[%c[N]+20:%c[N]+23], 0\n\t" \
        "v_sub_f32 %[m], %[la], %[m]\n\t" \
        "v_exp_f32 %[e], %[m]\n\t" \
        "v_mfma_f32_16x16x32_bf16 %[L2], %[a0], a[%c[N]+32:%c[N]+35], 0\n\t" \
        "s_nop 0\n\t" \
        "v_add_f32_dpp %[m], %[e], %[e] quad_perm:[1,0,3,2] row_mask:0xf bank_mask:0xf\n\t" \
        "v_mfma_f32_16x16x32_bf16 %[H2], %[a0], a[%c[N]+36:%c[N]+39], 0\n\t" \
        "s_nop 0\n\t" \
        "v_add_f32_dpp %[m], %[m], %[m] quad_perm:[2,3,0,1] row_mask:0xf bank_mask:0xf\n\t" \
        "v_mfma_f32_16x16x32_bf16 %[L3], %[a0], a[%c[N]+48:%c[N]+51], 0\n\t" \
        "s_nop 0\n\t" \
        "v_add_f32_dpp %[m], %[m], %[m] row_half_mirror row_mask:0xf bank_mask:0xf\n\t" \
        "v_mfma_f32_16x16x32_bf16 %[H3], %[a0], a[%c[N]+52:%c[N]+55], 0\n\t" \
        "v_rcp_f32 %[m], %[m]\n\t" \
        "v_mfma_f32_16x16x32_bf16 %[L0], %[a1], a[%c[N]+8:%c[N]+11], %[L0]\n\t" \
        "v_mul_f32 %[e], %[e], %[m]\n\t" \
        "ds_write_b32 %[aw], %[e]\n\t" \
        "v_mfma_f32_16x16x32_bf16 %[H0], %[a1], a[%c[N]+12:%c[N]+15], %[H0]\n\t" \
        "ds_read_b128 %[wt], %[ar]\n\t" \
        "v_mfma_f32_16x16x32_bf16 %[L1], %[a1], a[%c[N]+24:%c[N]+27], %[L1]\n\t" \
        "v_mfma_f32_16x16x32_bf16 %[H1], %[a1], a[%c[N]+28:%c[N]+31], %[H1]\n\t" \
        "v_mfma_f32_16x16x32_bf16 %[L2], %[a1], a[%c[N]+40:%c[N]+43], %[L2]\n\t" \
        "v_mfma_f32_16x16x32_bf16 %[H2], %[a1], a[%c[N]+44:%c[N]+47], %[H2]\n\t" \
        "v_mfma_f32_16x16x32_bf16 %[L3], %[a1], a[%c[N]+56:%c[N]+59], %[L3]\n\t" \
        "v_mfma_f32_16x16x32_bf16 %[H3], %[a1], a[%c[N]+60:%c[N]+63], %[H3]\n\t" \
        "s_waitcnt lgkmcnt(0)\n\t" \
        "s_nop 6\n\t" "" \
        : [L0] "=&v"(aL[0]), [H0] "=&v"(aH[0]), [L1] "=&v"(aL[1]), [H1] "=&v"(aH[1]), [L2] "=&v"(aL[2]), [H2] "=&v"(aH[2]), [L3] "=&v"(aL[3]), [H3] "=&v"(aH[3]), \
          [wt] "=&v"(wt), [m] "=&v"(tm), [e] "=&v"(te) \
        : [a0] "v"(oa0), [a1] "v"(oa1), [la] "v"(la), [aw] "v"(a_wtw), [ar] "v"(a_wtr), [N] "i"(NB) : "memory")
__device__ __forceinline__ float max3f(float a, float b, float c) { float r; asm("v_max3_f32 %0, %1, %2, %3" : "=v"(r) : "v"(a), "v"(b), "v"(c)); return r; }
__device__ __forceinline__ float max2f(float a, float b) { float r; asm("v_max_f32 %0, %1, %2" : "=v"(r) : "v"(a), "v"(b)); return r; }
__device__ __forceinline__ float x16_maxf(float x) {
    auto s = __builtin_amdgcn_permlane16_swap(__float_as_uint(x), __float_as_uint(x), false, false);
    x = max2f(__uint_as_float(s[0]), __uint_as_float(s[1]));
    auto t = __builtin_amdgcn_permlane32_swap(__float_as_uint(x), __float_as_uint(x), false, false);
    return max2f(__uint_as_float(t[0]), __uint_as_float(t[1]));
}
constexpr unsigned OOB = 0x80000000u;
template <int CTRL> __device__ __forceinline__ void dpp_acc(float& x) { x += dpp<CTRL>(x); asm("" : "+v"(x)); }
template <int CTRL> __device__ __forceinline__ void dpp_mx(float& x) { x = max2f(x, dpp<CTRL>(x)); }
#define UNLIKELY(x) __builtin_expect(!!(x), 0)
#define BAR() do { asm volatile("s_waitcnt lgkmcnt(0)" ::: "memory"); __builtin_amdgcn_s_barrier(); asm volatile("" ::: "memory"); } while (0)
#define VMWAIT(n) asm volatile("s_waitcnt vmcnt(" #n ")" ::: "memory")
template <int N> __device__ __forceinline__ void delay() {
#pragma unroll
    for (int i = 0; i < N; ++i) asm volatile("s_nop 15");
}

__device__ __forceinline__ void dma16(const unsigned char* sbase, unsigned voff, unsigned lds) {
    unsigned keep;
    asm volatile("s_mov_b32 %0, m0\n\ts_mov_b32 m0, %3\n\ts_nop 2\n\tglobal_load_lds_dwordx4 %1, %2" W_POL "\n\ts_mov_b32 m0, %0\n\ts_waitcnt vmcnt(%c4)"
                 : "=&s"(keep) : "v"(voff), "s"(sbase), "s"(lds), "i"(PACE - 1) : "memory");
}
__device__ __forceinline__ void dma16c(const unsigned char* sbase, unsigned voff, unsigned lds) {
    unsigned keep;
    asm volatile("s_mov_b32 %0, m0\n\ts_mov_b32 m0, %3\n\ts_nop 2\n\tglobal_load_lds_dwordx4 %1, %2\n\ts_mov_b32 m0, %0"
                 : "=&s"(keep) : "v"(voff), "s"(sbase), "s"(lds) : "memory");
}
__device__ __forceinline__ void dma16s(const unsigned char* sbase, unsigned voff, unsigned lds) {
    unsigned keep;
    asm volatile("s_mov_b32 %0, m0\n\ts_mov_b32 m0, %3\n\ts_nop 2\n\tglobal_load_lds_dwordx4 %1, %2 sc1\n\ts_mov_b32 m0, %0"
                 : "=&s"(keep) : "v"(voff), "s"(sbase), "s"(lds) : "memory");
}
__device__ __forceinline__ void dma16x4(const unsigned char* sbase, unsigned v0, unsigned v1, unsigned v2, unsigned v3, unsigned lds) {
    unsigned keep;
    asm volatile("s_mov_b32 %0, m0\n\ts_mov_b32 m0, %6\n\ts_nop 2\n\t"
                 "global_load_lds_dwordx4 %1, %5" KV_POL "\n\ts_waitcnt vmcnt(%c7)\n\tglobal_load_lds_dwordx4 %2, %5 offset:1024" KV_POL "\n\ts_waitcnt vmcnt(%c7)\n\t"
                 "global_load_lds_dwordx4 %3, %5 offset:2048" KV_POL "\n\ts_waitcnt vmcnt(%c7)\n\tglobal_load_lds_dwordx4 %4, %5 offset:3072" KV_POL "\n\ts_waitcnt vmcnt(%c7)\n\ts_mov_b32 m0, %0"
                 : "=&s"(keep) : "v"(v0), "v"(v1), "v"(v2), "v"(v3), "s"(sbase), "s"(lds), "i"(PACE - 1) : "memory");
}
template <class T> __device__ __forceinline__ T opq_s(T v) { asm volatile("" : "+s"(v)); return v; }
template <class T> __device__ __forceinline__ T opq_v(T v) { asm volatile("" : "+v"(v)); return v; }
#define BLD4(dst, voff, soff, imm) asm volatile("buffer_load_dwordx4 %0, %1, %2, %3 offen offset:" #imm " sc1" : "=&v"(dst) : "v"(voff), "s"(rsv), "s"(soff) : "memory")
#define BLD2(dst, voff, soff, imm) asm volatile("buffer_load_dwordx2 %0, %1, %2, %3 offen offset:" #imm " sc1" : "=&v"(dst) : "v"(voff), "s"(rsv), "s"(soff) : "memory")
#define BLD1(dst, voff, soff, imm) asm volatile("buffer_load_dword %0, %1, %2, %3 offen offset:" #imm " sc1" : "=&v"(dst) : "v"(voff), "s"(rsv), "s"(soff) : "memory")
#define BLD4W(dst, voff, soff) asm volatile("buffer_load_dwordx4 %0, %1, %2, %3 offen sc1\n\ts_waitcnt vmcnt(0)" : "=&v"(dst) : "v"(voff), "s"(rsv), "s"(soff) : "memory")
#define BLD2W(dst, voff, soff) asm volatile("buffer_load_dwordx2 %0, %1, %2, %3 offen sc1\n\ts_waitcnt vmcnt(0)" : "=&v"(dst) : "v"(voff), "s"(rsv), "s"(soff) : "memory")
#define BLDOW(a0, a1, la, v0, v1, soff, soffl) asm volatile("buffer_load_dwordx4 %0, %3, %5, %6 offen sc1\n\tbuffer_load_dwordx4 %1, %3, %5, %6 offen offset:128 sc1\n\tbuffer_load_dword %2, %4, %5, %7 offen sc1\n\ts_waitcnt vmcnt(0)" \
    : "=&v"(a0), "=&v"(a1), "=&v"(la) : "v"(v0), "v"(v1), "s"(rsv), "s"(soff), "s"(soffl) : "memory")
#define BLDXW(a0, a1, v0, v1, soff) asm volatile("buffer_load_dwordx4 %0, %2, %4, %5 offen sc1\n\tbuffer_load_dwordx4 %1, %3, %4, %5 offen sc1\n\ts_waitcnt vmcnt(0)" \
    : "=&v"(a0), "=&v"(a1) : "v"(v0), "v"(v1), "s"(rsv), "s"(soff) : "memory")
#define XLO(a, c) (bflo(a) + (c))
#define XHI(a, c) (bfhi(a) + (c))
__device__ __forceinline__ float acc_lo(unsigned a, float c) { return __builtin_amdgcn_fdot2_f32_bf16(__builtin_bit_cast(bf16x2, a), __builtin_bit_cast(bf16x2, 0x00003F80u), c, false); }
__device__ __forceinline__ float acc_hi(unsigned a, float c) { return __builtin_amdgcn_fdot2_f32_bf16(__builtin_bit_cast(bf16x2, a), __builtin_bit_cast(bf16x2, 0x3F800000u), c, false); }

__device__ __forceinline__ v4u tr2(LAS unsigned char* p0, LAS unsigned char* p1) {
    typedef short v4i16_t __attribute__((ext_vector_type(4)));
    v4i16_t a = __builtin_amdgcn_ds_read_tr16_b64_v4i16((LAS v4i16_t*)p0);
    v4i16_t b = __builtin_amdgcn_ds_read_tr16_b64_v4i16((LAS v4i16_t*)p1);
    v2u x = __builtin_bit_cast(v2u, a), y = __builtin_bit_cast(v2u, b);
    return v4u{x.x, x.y, y.x, y.y};
}

#define STAMP(i) do {} while (0)

#define XT(i) do {} while (0)
#define TS(i) do {} while (0)
struct Res { v4u kf[8], vf[8]; float cs; };
template <int V> using IC = std::integral_constant<int, V>;

__global__ void __launch_bounds__(256, 1) mega(Params p) {
    unsigned long long entry_pc;
    asm volatile("s_getpc_b64 %0" : "=s"(entry_pc));
    extern __shared__ __attribute__((aligned(16))) unsigned char smem[];
    LAS unsigned char* L = (LAS unsigned char*)smem;
    const int c = blockIdx.x, tid = threadIdx.x, lane = tid & 63, w = __builtin_amdgcn_readfirstlane(tid >> 6);
    const int kvh = c & 7, s = (c >> 3) & 7, b = c >> 6, grp = b * 8 + kvh;
    const int g = lane >> 4, r16 = lane & 15, half = lane >> 5, i32 = lane & 31;
    const __amdgpu_buffer_rsrc_t rs = __builtin_amdgcn_make_buffer_rsrc(p.ws, 0, WS_END, 0x00020000);
#define ST4(v, off) __builtin_amdgcn_raw_buffer_store_b128((v), rs, (off), 0, AUX_SC1)
#define ST2(v, off) __builtin_amdgcn_raw_buffer_store_b64((v), rs, (off), 0, AUX_SC1)
#define ST1(v, off) __builtin_amdgcn_raw_buffer_store_b32((v), rs, (off), 0, AUX_SC1)
#define ST2S(v, voff, soff) __builtin_amdgcn_raw_buffer_store_b64((v), rs, (voff), (soff), AUX_SC1)
#define ST1S(v, voff, soff) __builtin_amdgcn_raw_buffer_store_b32((v), rs, (voff), (soff), AUX_SC1)
    KEEP_ACC();
    STAMP(0);
    unsigned tsv = 0; (void)tsv;
    unsigned tsx = 0; (void)tsx;
    XT(0);
    TS(0);
    const unsigned long long wsa = (unsigned long long)p.ws;
    const v4u rsv = {(unsigned)wsa, (unsigned)(wsa >> 32) & 0xFFFFu, (unsigned)WS_END, 0x00020000u};

    auto stage_w = [&](const u16* wmat, const unsigned ld, const unsigned coloff, const int u, const unsigned ldsb, const int h) {
        const unsigned ln = opq_v((unsigned)lane);
        const unsigned wrow = ln >> 3, wpos = ln & 7, wf = (wrow >> 1) & 1;
        const unsigned v0 = wrow * ld + ((((wpos >> 1) ^ wf) << 1) | (wpos & 1)) * 16;
        const unsigned v1 = wrow * ld + ((((wpos >> 1) ^ (wf | 2)) << 1) | (wpos & 1)) * 16;
        const unsigned char* wb = (const unsigned char*)(opq_s(wmat) + coloff) + (size_t)(u * 128 + h * 64) * ld;
        const int bb = opq_s(b);
#pragma unroll 1
        for (int i = 0; i < 8; i += 2) {
            const int pc = (i + 2 * bb) & 7;
            dma16(wb + (size_t)(pc * 8) * ld, v0, ldsb + h * 8192 + pc * 1024);
            dma16(wb + (size_t)(pc * 8 + 8) * ld, v1, ldsb + h * 8192 + pc * 1024 + 1024);
        }
    };
    const unsigned kvrow = (unsigned)((b * HKV + kvh) * SK + (SK - W) + s * 128);
    auto stage_k = [&](const u16* cache, const int u, const unsigned ldsb, const int h0, const int h1) {
        const unsigned ln = opq_v((unsigned)lane), gg = ln >> 4, rr = ln & 15;
        const unsigned char* sb = (const unsigned char*)opq_s(cache) + (size_t)(kvrow + u * 32) * 256;
        const unsigned v0 = gg * 256 + (rr ^ gg) * 16, v1 = gg * 256 + (rr ^ (4 + gg)) * 16, v2 = gg * 256 + (rr ^ (8 + gg)) * 16, v3 = gg * 256 + (rr ^ (12 + gg)) * 16;
        if (h0 == 0) dma16x4(sb, v0, v1, v2, v3, ldsb);
        if (h1 == 2) dma16x4(sb + 4096, v0, v1, v2, v3, ldsb + 4096);
    };
    auto stage_v = [&](const u16* cache, const int u, const unsigned ldsb) {
        const unsigned ln = opq_v((unsigned)lane), gg = ln >> 4, rr = ln & 15;
        const unsigned char* sb = (const unsigned char*)opq_s(cache) + (size_t)(kvrow + u * 32) * 256;
        const unsigned v0 = gg * 256 + ((((rr >> 1) ^ gg) << 1) | (rr & 1)) * 16, v1 = gg * 256 + ((((rr >> 1) ^ (4 + gg)) << 1) | (rr & 1)) * 16;
        dma16x4(sb, v0, v1, v0, v1, ldsb);
        dma16x4(sb + 4096, v0, v1, v0, v1, ldsb + 4096);
    };
    auto consume_w = [&](auto NC, const unsigned ldsb) {
        constexpr int N = decltype(NC)::value;
        const unsigned ln = opq_v((unsigned)lane), gg = ln >> 4, qq = (ln & 15) >> 2, pp = ln & 3;
        const unsigned f = (qq >> 1) | ((gg & 1) << 1);
        const unsigned base = ldsb + (8 * gg + qq) * 128 + (pp >> 1) * 16 + (pp & 1) * 8;
        tr_col<N>(base + (0 ^ f) * 32); tr_col<N + 16>(base + (1 ^ f) * 32); tr_col<N + 32>(base + (2 ^ f) * 32); tr_col<N + 48>(base + (3 ^ f) * 32);
        asm volatile("s_waitcnt lgkmcnt(0)" ::: "memory");
    };
#define LOAD_K(R, cache) do { \
        const unsigned ln_ = opq_v((unsigned)lane); \
        const unsigned char* kb_ = (const unsigned char*)opq_s(cache) + (size_t)(kvrow + opq_s(w) * 32) * 256; \
        const unsigned v_ = (ln_ & 15) * 256 + (ln_ >> 4) * 16, v2_ = v_ + 4096; \
        asm volatile("s_nop 4\n\tglobal_load_dwordx4 %0, %8, %10" KV_POL "\n\tglobal_load_dwordx4 %1, %8, %10 offset:64" KV_POL "\n\tglobal_load_dwordx4 %2, %8, %10 offset:128" KV_POL "\n\tglobal_load_dwordx4 %3, %8, %10 offset:192" KV_POL "\n\t" \
                     "global_load_dwordx4 %4, %9, %10" KV_POL "\n\tglobal_load_dwordx4 %5, %9, %10 offset:64" KV_POL "\n\tglobal_load_dwordx4 %6, %9, %10 offset:128" KV_POL "\n\tglobal_load_dwordx4 %7, %9, %10 offset:192" KV_POL \
                     : "=&v"(R.kf[0]), "=&v"(R.kf[1]), "=&v"(R.kf[2]), "=&v"(R.kf[3]), "=&v"(R.kf[4]), "=&v"(R.kf[5]), "=&v"(R.kf[6]), "=&v"(R.kf[7]) \
                     : "v"(v_), "v"(v2_), "s"(kb_) : "memory"); } while (0)
#define PIN_K(R) asm volatile("; COVERED_K" : "+v"(R.kf[0]), "+v"(R.kf[1]), "+v"(R.kf[2]), "+v"(R.kf[3]), "+v"(R.kf[4]), "+v"(R.kf[5]), "+v"(R.kf[6]), "+v"(R.kf[7]))
    auto consume_k = [&](v4u* dst, const unsigned ldsb) {
        const unsigned ln = opq_v((unsigned)lane), gg = ln >> 4, rr = ln & 15;
#pragma unroll
        for (int t = 0; t < 4; ++t) {
            LAS unsigned char* a = L + ldsb + rr * 256 + (((4 * t + gg) ^ rr) * 16);
            dst[t] = *(LAS v4u*)a; dst[4 + t] = *(LAS v4u*)(a + 4096);
        }
    };
    auto consume_v = [&](v4u* dst, const unsigned off) {
        const unsigned ln = opq_v((unsigned)lane), gg = ln >> 4, qq = (ln & 15) >> 2, pp = ln & 3;
        const unsigned vrow = 4 * gg + qq;
#pragma unroll
        for (int i = 0; i < 2; ++i) {
            LAS unsigned char* a = L + off + vrow * 256 + (((unsigned)(2 * w + i) ^ (vrow & 7)) * 32) + (pp >> 1) * 16 + (pp & 1) * 8;
#pragma unroll
            for (int t = 0; t < 4; ++t) dst[t * 2 + i] = tr2(a + t * RW, a + t * RW + 4096);
        }
    };
    const unsigned wq_col = (unsigned)(kvh * 512 + s * 64), wo_col = (unsigned)(kvh * 512 * 512 + s * 64);
    const unsigned myr = (unsigned)(w * RW);

    Res RA, RB;
#define RC(x)

    const unsigned xcol = (unsigned)(128 * w + 8 * r16 + 4 * (g >> 1) + 2 * (g & 1));
    unsigned xpk = 0;
    v2u gen = {0u, 0u};
    if (CODE_KB && w == 3) {
        const int piece = (c >> 3) % CODE_KB;
        dma16c((const unsigned char*)(entry_pc & ~255ull) + piece * 1024, (unsigned)lane * 16, L_TRASH);
    }
    if (w == 0) {
        dma16s(p.ws + WS_GEN, opq_v((unsigned)lane) * 16, L_GENB);
        dma16((const unsigned char*)p.x + b * 1024, opq_v((unsigned)lane) * 16, L_XS);
    }
    stage_w(p.wq0, 8192, wq_col, w, myr + U0, 0); stage_w(p.wq0, 8192, wq_col, w, myr + U0, 1);
    TS(15);
    LOAD_K(RA, p.kc0);
    stage_v(p.vc0, w, myr + U1 + 8192);
    TS(30);
    TS(45);
    ((LAS unsigned*)(L + L_ON))[lane] = 0x3F803F80u;

    const unsigned xg_v0 = (unsigned)(g * XB_K + w * 256 + r16 * 16), xg_v1 = xg_v0 + 4 * XB_K;
    const unsigned vq_st = ((lane & 1) == 0 && g == w) ? (unsigned)(lane * 2) : OOB;
    const unsigned vo_st = r16 < 4 ? (unsigned)(r16 * 256 + g * 8) : OOB;
    const unsigned vl_st = (lane < 4) ? (unsigned)(lane * 32) : OOB;
    const unsigned vx_st = (unsigned)((2 * w + half) * XB_R + i32 * 4);

    auto x_sum = [&](const v4u& a0, const v4u& a1, const bool active, float& o0, float& o1) -> bool {
        float v[8];
#pragma unroll
        for (int j = 0; j < 4; ++j) { v[2 * j] = XLO(a1[j], bflo(a0[j])); v[2 * j + 1] = XHI(a1[j], bfhi(a0[j])); }
        float u[4];
#pragma unroll
        for (int j = 0; j < 4; ++j) u[j] = swap32_add(v[j], v[j + 4]);
        o0 = swap16_add(u[0], u[2]); o1 = swap16_add(u[1], u[3]);
        const bool ok = !active || !__builtin_isunordered(o0, o1);
        if (__all(ok)) return true;
        const unsigned m = max(mx4(a0), mx4(a1));
        return __all(!active || m != EMPTY);
    };
    auto hello_ok = [&](const v2u& h) -> bool { return __all(h.x == gen.x && h.y == gen.y); };
    bool hello64 = false;

#pragma unroll 1
    for (int lp = 0; lp < NLAY; ++lp) {
        const int l = opq_s(lp < 0 ? 0 : lp);
        const bool live = lp >= 0;
        const bool TA = (lp & 1) == 0;
        const bool f0 = lp == 0, f1 = lp == 1;
        const int so_q = WS_QB + l * QB_L + grp * 1024, so_o = WS_OB + l * OB_L + grp * OB_G, so_l = WS_LB + l * LB_L + grp * 128, so_x = WS_XB + l * XB_L + b * XB_B;
        if (f0) {
            TS(60);
            VMWAIT(16);
            BAR();
            {
                const v2u g0 = *(LAS v2u*)(L + L_GENB + b * 256);
                const unsigned long long g1 = (((unsigned long long)g0.y << 32) | g0.x) + 1ull;
                gen = v2u{(unsigned)g1, (unsigned)(g1 >> 32)};
            }
            xpk = *(LAS unsigned*)(L + L_XS + xcol * 2);
        }
        STAMP(1 + l * 8 + 0);
        if (f0) XT(4);
        if (lp == 2) XT(28);
        KEEP_ACC();
        TS(1 + l * 15 + 0);
        if (lp > 0) *(LAS unsigned*)(L + L_XS + xcol * 2) = xpk;
        float rstd, tq;
        f32x4 acc[4];
        {
            LAS unsigned char* xb = L + (((f0 || f1) && r16 == 1) ? L_ON : L_XS + w * 256) + g * 16;
            v4u f[4];
#pragma unroll
            for (int t = 0; t < 4; ++t) f[t] = *(LAS v4u*)(xb + t * 64);
            SB();
            {
                const float x0 = bflo(xpk), x1 = bfhi(xpk);
                float p1 = x0 + x1, p2 = x0 * x0 + x1 * x1;
                dpp_acc<XOR1>(p1); dpp_acc<XOR1>(p2); dpp_acc<XOR2>(p1); dpp_acc<XOR2>(p2);
                dpp_acc<HMIR>(p1); dpp_acc<HMIR>(p2); dpp_acc<MIR>(p1); dpp_acc<MIR>(p2);
                *(LAS f32x2*)(L + L_ST + (w * 4 + g) * 8) = f32x2{p1, p2};
            }
            SB();
            if (f0) consume_w(IC<0>{}, myr + U0);
            TS(1 + l * 15 + 1);
            const unsigned a_st = (unsigned)(L_ST + r16 * 8);
            const int livei = opq_s(lp);
            if (TA) QBLOCK(0); else QBLOCK(128);
            LAS float* rq = (LAS float*)(L + L_RQ) + w * 64 + r16;
            rq[0] = acc[0][0]; rq[16] = acc[1][0]; rq[32] = acc[2][0]; rq[48] = acc[3][0];
            if (f0 || f1) {
                LAS float* rc = (LAS float*)(L + (g == 0 ? L_RC + w * 256 : L_TRASH + g * 256)) + r16;
                rc[0] = acc[0][1]; rc[16] = acc[1][1]; rc[32] = acc[2][1]; rc[48] = acc[3][1];
            }
        }
        asm volatile("s_waitcnt lgkmcnt(0)" ::: "memory");
        if (live) __builtin_amdgcn_s_barrier();
        asm volatile("" ::: "memory");
        TS(1 + l * 15 + 2);
        KEEP_ACC();
        v4u qa0 = {0u, 0u, 0u, 0u};
        {
            LAS float* rq = (LAS float*)(L + L_RQ) + lane;
            const float raw = (rq[0] + rq[64]) + (rq[128] + rq[192]);
            if (f0 || f1) {
                LAS float* rc = (LAS float*)(L + L_RC) + lane;
                const float cs = (rc[0] + rc[64]) + (rc[128] + rc[192]);
                if (f0) RA.cs = cs; else RB.cs = cs;
            }
            const float q = raw * rstd + tq * ((lp & 1) ? RB.cs : RA.cs);
            const unsigned qo = noempty(cvtpk(q, dpp<XOR1>(q)));
            if (live) {
                const int soff = opq_s(so_q + w * 256);
                const unsigned voff = (unsigned)(r16 * 16);
                if (f0) {
                    ST2S((v2u{qo, gen.x}), vq_st == OOB ? OOB : (unsigned)((lane >> 1) * 8), WS_QT + grp * 2048 + s * 256);
                    XT(5);
                    TS(1 + l * 15 + 3);
                    stage_w(p.wo0, 1024, wo_col, w, myr + U0, 0); stage_w(p.wo0, 1024, wo_col, w, myr + U0, 1);
                    {
                        const v4u e = {EMPTY, EMPTY, EMPTY, EMPTY};
                        const unsigned ln = opq_v((unsigned)lane);
#pragma unroll
                        for (int l4 = 0; l4 < NLAY; l4 += 4) {
                            ST4(e, WS_OB + (l4 + w) * OB_L + grp * OB_G + s * OB_P + ln * 16);
                            ST4(e, WS_XB + (l4 + w) * XB_L + b * XB_B + (ln >> 3) * XB_R + kvh * XB_K + s * 128 + (ln & 7) * 16);
                        }
                        if (w == 0) ST4(e, (ln >> 3) < NLAY ? (unsigned)(WS_QB + (ln >> 3) * QB_L + grp * 1024 + s * 128 + (ln & 7) * 16) : OOB);
                        if (w == 1) ST1(EMPTY, (ln >> 2) < NLAY ? (unsigned)(WS_LB + (ln >> 2) * LB_L + grp * 128 + (ln & 3) * 32 + s * 4) : OOB);
                    }
                    XT(6);
                    const int sofft = opq_s(WS_QT + grp * 2048 + w * 512);
                    const unsigned vofft = (unsigned)(i32 * 16);
                    v4u t;
                    BLD4(t, vofft, sofft, 0);
                    LOAD_K(RB, p.kc1);
                    stage_v(p.vc1, w, myr + U1);
                    asm volatile("s_waitcnt vmcnt(16) ; COVER" : "+v"(t) :: "memory");
                    PIN_K(RA);
                    if (UNLIKELY(!__all(t.y == gen.x && t.w == gen.x))) { for (unsigned sp = 0; sp < SPIN_CAP; ++sp) { BLD4W(t, vofft, sofft); if (__all(t.y == gen.x && t.w == gen.x)) break; } }
                    *(LAS v2u*)(L + L_QS + w * 256 + i32 * 8) = v2u{t.x, t.z};
                    XT(8);
                } else {
                    ST1S(qo, vq_st, so_q + s * 128);
                    TS(1 + l * 15 + 3);
                    delay<DLY_Q>();
                    BLD4W(qa0, voff, soff);
                }
                if (!f0 && UNLIKELY(!__all(mx4(qa0) != EMPTY))) {
                    for (unsigned sp = 0; sp < SPIN_CAP; ++sp) { RC(rcq); BLD4W(qa0, voff, soff); if (__all(mx4(qa0) != EMPTY)) break; }
                }
            }
        }
        TS(1 + l * 15 + 4);
        KEEP_ACC();
        if (!f0) *(LAS v4u*)(L + L_QS + w * 256 + r16 * 16) = qa0;
        STAMP(1 + l * 8 + 2);
        asm volatile("s_waitcnt lgkmcnt(0)" ::: "memory");
        if (live) __builtin_amdgcn_s_barrier();
        asm volatile("" ::: "memory");
        TS(1 + l * 15 + 5);
        if (f0 && tid == 0) ST2(gen, WS_HELLO + (b * 64 + kvh * 8 + s) * 8);
        f32x4 sc0, sc1;
        {
            LAS unsigned char* qb = L + L_QS + (lane & 3) * 256 + g * 16;
            v4u qf[4];
#pragma unroll
            for (int t = 0; t < 4; ++t) qf[t] = *(LAS v4u*)(qb + t * 64);
            SB();
            if (TA) QKBLOCK(RA); else QKBLOCK(RB);
            SB();
            if (f0) consume_v(RA.vf, U1 + 8192);
            asm volatile("s_nop 7" : "+v"(sc0), "+v"(sc1));
        }
        TS(1 + l * 15 + 6);
        KEEP_ACC();
        constexpr float CS = 0.08838834764831845f * 1.4426950408889634f;
        float mw = max3f(max3f(sc0[0], sc0[1], sc0[2]), max3f(sc0[3], sc1[0], sc1[1]), max2f(sc1[2], sc1[3]));
        mw = x16_maxf(mw) * CS;
        float pe[8];
#pragma unroll
        for (int j = 0; j < 4; ++j) { pe[j] = __builtin_amdgcn_exp2f(sc0[j] * CS - mw); pe[4 + j] = __builtin_amdgcn_exp2f(sc1[j] * CS - mw); }
        {
            const v4u pf = {cvtpk(pe[0], pe[1]), cvtpk(pe[2], pe[3]), cvtpk(pe[4], pe[5]), cvtpk(pe[6], pe[7])};
            *(LAS v4u*)(L + L_PS + w * 256 + (lane & 3) * 64 + g * 16) = pf;
            SB();
            float lw = ((pe[0] + pe[1]) + (pe[2] + pe[3])) + ((pe[4] + pe[5]) + (pe[6] + pe[7]));
            lw = x16_sum(lw);
            *(LAS f32x2*)(L + L_ML + (lane & 3) * 32 + w * 8) = f32x2{mw, lw};
        }
        TS(1 + l * 15 + 7);
        KEEP_ACC();
        asm volatile("s_waitcnt lgkmcnt(0)" ::: "memory");
        if (live) __builtin_amdgcn_s_barrier();
        asm volatile("" ::: "memory");
        v4u oa0 = {0u, 0u, 0u, 0u}, oa1 = oa0; unsigned la = 0;
        {
            LAS unsigned char* pb = L + L_PS + (lane & 3) * 64 + g * 16;
            const f32x4 ml0 = *(LAS f32x4*)(L + L_ML + (lane & 3) * 32), ml1 = *(LAS f32x4*)(L + L_ML + (lane & 3) * 32 + 16);
            v4u pf[4];
#pragma unroll
            for (int t = 0; t < 4; ++t) pf[t] = *(LAS v4u*)(pb + t * 256);
            SB();
            const float M = max2f(max3f(ml0[0], ml0[2], ml1[0]), ml1[2]);
            const float a0 = __builtin_amdgcn_exp2f(ml0[0] - M), a1 = __builtin_amdgcn_exp2f(ml0[2] - M);
            const float a2 = __builtin_amdgcn_exp2f(ml1[0] - M), a3 = __builtin_amdgcn_exp2f(ml1[2] - M);
            const float Ls = (a0 * ml0[1] + a1 * ml0[3]) + (a2 * ml1[1] + a3 * ml1[3]);
            const float inv = __builtin_amdgcn_rcpf(Ls);
            float lam = M + __builtin_amdgcn_logf(Ls);
            float b0 = a0 * inv, b1 = a1 * inv, b2 = a2 * inv, b3 = a3 * inv;
            PIN(b0); PIN(b1); PIN(b2); PIN(b3); PIN(lam);
            SB();
            f32x4 oa[4][2];
            if (TA) PVBLOCK(RA); else PVBLOCK(RB);
            TS(1 + l * 15 + 8);
            f32x4 o0 = oa[0][0] * b0, o1 = oa[0][1] * b0;
            o0 += oa[1][0] * b1; o1 += oa[1][1] * b1;
            o0 += oa[2][0] * b2; o1 += oa[2][1] * b2;
            o0 += oa[3][0] * b3; o1 += oa[3][1] * b3;
            const v2u w0 = {noempty(cvtpk(o0[0], o0[1])), noempty(cvtpk(o0[2], o0[3]))};
            const v2u w1 = {noempty(cvtpk(o1[0], o1[1])), noempty(cvtpk(o1[2], o1[3]))};
            if (live) {
                const int ob = so_o + s * OB_P;
                ST2S(w0, vo_st, ob + w * 64);
                ST2S(w1, vo_st, ob + w * 64 + 32);
                if (w == 0) ST1S(noempty(__float_as_uint(lam)), vl_st, so_l + s * 4);
                STAMP(1 + l * 8 + 3);
                TS(1 + l * 15 + 9);
                const int soff = opq_s(so_o + w * 256), soffl = opq_s(so_l + w * 32);
                const unsigned voff = (unsigned)((r16 & 7) * OB_P + (r16 >> 3) * 64 + g * 16);
                const unsigned voffl = (unsigned)((lane & 7) * 4);
                if (f0) {
                    v2u h;
                    const int hoff = opq_s(WS_HELLO + (b * 64) * 8);
                    const unsigned hv = (unsigned)(lane * 8);
                    const bool mine = (lane >> 3) == kvh;
                    BLD2(h, hv, hoff, 0);
                    consume_w(IC<64>{}, myr + U0);
                    asm volatile("s_waitcnt vmcnt(0) ; COVER" : "+v"(h) :: "memory");
                    PIN_K(RB);
                    if (UNLIKELY(!__all(!mine || (h.x == gen.x && h.y == gen.y)))) { for (unsigned sp = 0; sp < SPIN_CAP; ++sp) { BLD2W(h, hv, hoff); if (__all(!mine || (h.x == gen.x && h.y == gen.y))) break; } }
                    hello64 = hello_ok(h);
                    BLD4(oa0, voff, soff, 0); BLD4(oa1, voff, soff, 128); BLD1(la, voffl, soffl, 0);
                    stage_w(p.wq1, 8192, wq_col, w, myr + U0, 0); stage_w(p.wq1, 8192, wq_col, w, myr + U0, 1);
                    asm volatile("s_waitcnt vmcnt(16) ; COVER" : "+v"(oa0), "+v"(oa1), "+v"(la) :: "memory");
                } else if (f1) {
                    delay<DLY_O - 2>();
                    consume_w(IC<192>{}, myr + U1);
                    BLDOW(oa0, oa1, la, voff, voffl, soff, soffl);
                } else {
                    delay<DLY_O>();
                    BLDOW(oa0, oa1, la, voff, voffl, soff, soffl);
                }
                if (UNLIKELY(!__all(max(max(mx4(oa0), mx4(oa1)), la) != EMPTY))) {
                    for (unsigned sp = 0; sp < SPIN_CAP; ++sp) { RC(rco); BLDOW(oa0, oa1, la, voff, voffl, soff, soffl); if (__all(max(max(mx4(oa0), mx4(oa1)), la) != EMPTY)) break; }
                }
            }
        }
        STAMP(1 + l * 8 + 4);
        TS(1 + l * 15 + 10);
        KEEP_ACC();
        {
            f32x4 aL[4], aH[4];
            f32x4 wt; float tm, te;
            const unsigned a_wtw = (unsigned)(L_WT + w * 32 + (lane & 7) * 4), a_wtr = (unsigned)(L_WT + w * 32 + (g & 1) * 16);
            if (TA) OBLOCK(64); else OBLOCK(192);
            const f32x2 z2 = {0.f, 0.f};
            const f32x2 wL0 = half ? z2 : f32x2{wt[0], wt[1]}, wL1 = half ? z2 : f32x2{wt[2], wt[3]};
            const f32x2 wH0 = half ? f32x2{wt[0], wt[1]} : z2, wH1 = half ? f32x2{wt[2], wt[3]} : z2;
            TS(1 + l * 15 + 11);
            float v[4];
#pragma unroll
            for (int n = 0; n < 4; ++n) {
                f32x2 t = f32x2{aL[n][0], aL[n][1]} * wL0;
                t += f32x2{aL[n][2], aL[n][3]} * wL1; t += f32x2{aH[n][0], aH[n][1]} * wH0; t += f32x2{aH[n][2], aH[n][3]} * wH1;
                v[n] = t.x + t.y;
            }
            const float u0 = swap32_add(v[0], v[1]), u1 = swap32_add(v[2], v[3]);
            const float tot = swap16_add(u0, u1);
            *((LAS float*)(L + L_RX) + w * 64 + ((g & 1) * 2 + (g >> 1)) * 16 + r16) = tot;
        }
        asm volatile("s_waitcnt lgkmcnt(0)" ::: "memory");
        if (live) __builtin_amdgcn_s_barrier();
        asm volatile("" ::: "memory");
        TS(1 + l * 15 + 12);
        KEEP_ACC();
        {
            LAS unsigned char* rx = L + L_RX + i32 * 8;
            const f32x2 sum = (*(LAS f32x2*)(rx) + *(LAS f32x2*)(rx + 256)) + (*(LAS f32x2*)(rx + 512) + *(LAS f32x2*)(rx + 768));
            const unsigned ov = noempty(cvtpk(sum.x, sum.y));
            if (live) {
                ST1S(ov, vx_st, so_x + kvh * XB_K + s * 128);
                STAMP(1 + l * 8 + 5);
                TS(1 + l * 15 + 13);
                const bool fin = lp == NLAY - 1;
                const bool act = !fin || (s == 0 && w == (kvh >> 1) && (r16 >> 3) == (kvh & 1));
                if (!fin || (s == 0 && w == (kvh >> 1))) {
                    const int soff = opq_s(so_x + kvh * XB_R);
                    const unsigned v0 = act ? xg_v0 : OOB, v1 = act ? xg_v1 : OOB;
                    v4u a0, a1;
                    if (f0) {
                        XT(14);
                        if (UNLIKELY(!hello64)) {
                            v2u h;
                            const int hoff = opq_s(WS_HELLO + (b * 64) * 8);
                            const unsigned hv = (unsigned)(lane * 8);
                            for (unsigned sp = 0; sp < SPIN_CAP; ++sp) { BLD2W(h, hv, hoff); if (hello_ok(h)) break; }
                        }
                        if ((c & 63) == 0 && tid == 0) ST2(gen, WS_GEN + b * 256);
                        consume_v(RB.vf, U1);
                        BAR();
                        delay<DLY_X - 6>();
                        BLD4(a0, v0, soff, 0); BLD4(a1, v1, soff, 0);
                        stage_w(p.wo1, 1024, wo_col, w, myr + U1, 0); stage_w(p.wo1, 1024, wo_col, w, myr + U1, 1);
                        asm volatile("s_waitcnt vmcnt(16) ; COVER" : "+v"(a0), "+v"(a1) :: "memory");
                        consume_w(IC<128>{}, myr + U0);
                        XT(17);
                    } else {
                        delay<DLY_X>();
                        BLDXW(a0, a1, v0, v1, soff);
                    }
                    float o0, o1;
                    if (UNLIKELY(!x_sum(a0, a1, act, o0, o1))) {
                        for (unsigned sp = 0; sp < SPIN_CAP; ++sp) { RC(rcx); BLDXW(a0, a1, v0, v1, soff); if (x_sum(a0, a1, act, o0, o1)) break; }
                    }
                    xpk = cvtpk(bflo(xpk) + o0, bfhi(xpk) + o1);
                    if (f0) XT(18);
                    if (f1) XT(27);
                    if (fin && act) *(GAS unsigned*)((GAS unsigned char*)p.out + (b * 512 + xcol) * 2) = xpk;
                }
            }
        }
    }
    STAMP(40);
}
}

extern "C" void kernel_launch(void* const* d_in, const int* in_sizes, int n_in, void* d_out, int out_size, void* d_ws, size_t ws_size, hipStream_t stream) {
    mk::Params p;
    p.x = (const u16*)d_in[0]; p.kc0 = (const u16*)d_in[1]; p.vc0 = (const u16*)d_in[2]; p.wq0 = (const u16*)d_in[3]; p.wo0 = (const u16*)d_in[4];
    p.kc1 = (const u16*)d_in[6]; p.vc1 = (const u16*)d_in[7]; p.wq1 = (const u16*)d_in[8]; p.wo1 = (const u16*)d_in[9];
    p.out = (u16*)d_out; p.ws = (unsigned char*)d_ws;
    (void)hipFuncSetAttribute((const void*)mk::mega, hipFuncAttributeMaxDynamicSharedMemorySize, mk::LDS_BYTES);
    hipLaunchKernelGGL(mk::mega, dim3(256), dim3(256), mk::LDS_BYTES, stream, p);
}
```

```cpp
#include <hip/hip_runtime.h>
#include <hip/hip_bf16.h>
#include <stdint.h>
#include <type_traits>

#define W_POL ""
#define KV_POL " nt"
#define NLAY 4
#define DLY_Q 6
#define DLY_O 6
#define DLY_X 14

#define GAS __attribute__((address_space(1)))
#define LAS __attribute__((address_space(3)))
typedef unsigned short u16;
typedef unsigned v4u __attribute__((ext_vector_type(4)));
typedef unsigned v2u __attribute__((ext_vector_type(2)));
typedef float f32x4 __attribute__((ext_vector_type(4)));
typedef float f32x2 __attribute__((ext_vector_type(2)));
typedef __bf16 bf16x2 __attribute__((ext_vector_type(2)));

namespace mk {
constexpr int HKV = 8, SK = 4096, DH = 128, W = 1024;
constexpr unsigned EMPTY = 0xFFFFFFFFu;
constexpr int AUX_SC1 = 16;
constexpr unsigned SPIN_CAP = 1u << 17;

constexpr int WS_GEN = 0;
constexpr int WS_HELLO = 1024;
constexpr int WS_QT = 4096;
constexpr int WS_QB = WS_QT + 32 * 2048;
constexpr int QB_L = 32 * 1024;
constexpr int WS_OB = WS_QB + NLAY * QB_L;
constexpr int OB_P = 1024, OB_G = 8 * OB_P, OB_L = 32 * OB_G;
constexpr int WS_LB = WS_OB + NLAY * OB_L;
constexpr int LB_L = 32 * 128;
constexpr int WS_XB = WS_LB + NLAY * LB_L;
constexpr int XB_K = 1024, XB_R = 8 * XB_K, XB_B = 8 * XB_R, XB_L = 4 * XB_B;
constexpr int WS_END = WS_XB + NLAY * XB_L;

constexpr int RW = 36864;
constexpr int U0 = 0, U1 = 16384;
constexpr int L_EX = 4 * RW;
constexpr int L_XS = L_EX;
constexpr int L_QS = L_XS + 1024;
constexpr int L_PS = L_QS + 1024;
constexpr int L_ON = L_PS + 1024;
constexpr int L_RQ = L_ON + 256;
constexpr int L_RC = L_RQ + 1024;
constexpr int L_RX = L_RC + 1024;
constexpr int L_ML = L_RX + 1024;
constexpr int L_ST = L_ML + 128;
constexpr int L_WT = L_ST + 128;
constexpr int L_TRASH = L_WT + 128;
constexpr int L_GENB = L_TRASH + 1024;
constexpr int L_END = L_GENB + 1024;
constexpr int LDS_BYTES = 160 * 1024;
static_assert(L_END <= LDS_BYTES, "lds");
static_assert((L_WT % 16) == 0 && (L_ST % 16) == 0 && (L_RQ % 16) == 0 && (L_TRASH % 16) == 0, "align");

struct Params {
    const u16* x; const u16* kc0; const u16* vc0; const u16* wq0; const u16* wo0;
    const u16* kc1; const u16* vc1; const u16* wq1; const u16* wo1;
    u16* out; unsigned char* ws;
};

__device__ __forceinline__ unsigned cvtpk(float lo, float hi) {
    bf16x2 r = {(__bf16)lo, (__bf16)hi};
    return __builtin_bit_cast(unsigned, r);
}
__device__ __forceinline__ float bflo(unsigned u) { return __builtin_bit_cast(float, u << 16); }
__device__ __forceinline__ float bfhi(unsigned u) { return __builtin_bit_cast(float, u & 0xFFFF0000u); }
template <int CTRL> __device__ __forceinline__ float dpp(float x) {
    return __builtin_bit_cast(float, __builtin_amdgcn_mov_dpp(__builtin_bit_cast(int, x), CTRL, 0xf, 0xf, true));
}
constexpr int XOR1 = 0xB1, XOR2 = 0x4E, HMIR = 0x141, MIR = 0x140;
__device__ __forceinline__ float x16_sum(float x) {
    auto s = __builtin_amdgcn_permlane16_swap(__float_as_uint(x), __float_as_uint(x), false, false);
    x = __uint_as_float(s[0]) + __uint_as_float(s[1]);
    auto t = __builtin_amdgcn_permlane32_swap(__float_as_uint(x), __float_as_uint(x), false, false);
    return __uint_as_float(t[0]) + __uint_as_float(t[1]);
}
__device__ __forceinline__ float swap32_add(float a, float b) {
    auto s = __builtin_amdgcn_permlane32_swap(__float_as_uint(a), __float_as_uint(b), false, false);
    return __uint_as_float(s[0]) + __uint_as_float(s[1]);
}
__device__ __forceinline__ float swap16_add(float a, float b) {
    auto s = __builtin_amdgcn_permlane16_swap(__float_as_uint(a), __float_as_uint(b), false, false);
    return __uint_as_float(s[0]) + __uint_as_float(s[1]);
}
__device__ __forceinline__ unsigned mx4(v4u v) { return max(max(v.x, v.y), max(v.z, v.w)); }
__device__ __forceinline__ unsigned noempty(unsigned u) { return min(u, 0xFFFFFFFEu); }
#define SB() __builtin_amdgcn_sched_barrier(0)
#define KEEP_ACC() asm volatile("" ::: "a0", "a1", "a2", "a3", "a4", "a5", "a6", "a7", "a8", "a9", "a10", "a11", "a12", "a13", "a14", "a15", "a16", "a17", "a18", "a19", "a20", "a21", "a22", "a23", "a24", "a25", "a26", "a27", "a28", "a29", "a30", "a31", "a32", "a33", "a34", "a35", "a36", "a37", "a38", "a39", "a40", "a41", "a42", "a43", "a44", "a45", "a46", "a47", "a48", "a49", "a50", "a51", "a52", "a53", "a54", "a55", "a56", "a57", "a58", "a59", "a60", "a61", "a62", "a63", "a64", "a65", "a66", "a67", "a68", "a69", "a70", "a71", "a72", "a73", "a74", "a75", "a76", "a77", "a78", "a79", "a80", "a81", "a82", "a83", "a84", "a85", "a86", "a87", "a88", "a89", "a90", "a91", "a92", "a93", "a94", "a95", "a96", "a97", "a98", "a99", "a100", "a101", "a102", "a103", "a104", "a105", "a106", "a107", "a108", "a109", "a110", "a111", "a112", "a113", "a114", "a115", "a116", "a117", "a118", "a119", "a120", "a121", "a122", "a123", "a124", "a125", "a126", "a127", "a128", "a129", "a130", "a131", "a132", "a133", "a134", "a135", "a136", "a137", "a138", "a139", "a140", "a141", "a142", "a143", "a144", "a145", "a146", "a147", "a148", "a149", "a150", "a151", "a152", "a153", "a154", "a155", "a156", "a157", "a158", "a159", "a160", "a161", "a162", "a163", "a164", "a165", "a166", "a167", "a168", "a169", "a170", "a171", "a172", "a173", "a174", "a175", "a176", "a177", "a178", "a179", "a180", "a181", "a182", "a183", "a184", "a185", "a186", "a187", "a188", "a189", "a190", "a191", "a192", "a193", "a194", "a195", "a196", "a197", "a198", "a199", "a200", "a201", "a202", "a203", "a204", "a205", "a206", "a207", "a208", "a209", "a210", "a211", "a212", "a213", "a214", "a215", "a216", "a217", "a218", "a219", "a220", "a221", "a222", "a223", "a224", "a225", "a226", "a227", "a228", "a229", "a230", "a231", "a232", "a233", "a234", "a235", "a236", "a237", "a238", "a239", "a240", "a241", "a242", "a243", "a244", "a245", "a246", "a247", "a248", "a249", "a250", "a251", "a252", "a253", "a254", "a255")
#define PIN(x) asm volatile("" : "+v"(x))
template <int N> __device__ __forceinline__ void tr_col(unsigned addr) {
    asm volatile("ds_read_b64_tr_b16 a[%c1:%c2], %0\n\tds_read_b64_tr_b16 a[%c3:%c4], %0 offset:512\n\t"
                 "ds_read_b64_tr_b16 a[%c5:%c6], %0 offset:4096\n\tds_read_b64_tr_b16 a[%c7:%c8], %0 offset:4608"
                 :: "v"(addr), "i"(N), "i"(N + 1), "i"(N + 2), "i"(N + 3), "i"(N + 4), "i"(N + 5), "i"(N + 6), "i"(N + 7) : "memory");
    asm volatile("ds_read_b64_tr_b16 a[%c1:%c2], %0 offset:8192\n\tds_read_b64_tr_b16 a[%c3:%c4], %0 offset:8704\n\t"
                 "ds_read_b64_tr_b16 a[%c5:%c6], %0 offset:12288\n\tds_read_b64_tr_b16 a[%c7:%c8], %0 offset:12800"
                 :: "v"(addr), "i"(N + 8), "i"(N + 9), "i"(N + 10), "i"(N + 11), "i"(N + 12), "i"(N + 13), "i"(N + 14), "i"(N + 15) : "memory");
}
#define QBLOCK(NB) asm volatile("v_mfma_f32_16x16x32_bf16 %[c0], %[f0], a[%c[N]+0:%c[N]+3], 0\n\t" \
        "v_mfma_f32_16x16x32_bf16 %[c1], %[f0], a[%c[N]+16:%c[N]+19], 0\n\t" \
        "s_waitcnt lgkmcnt(0)\n\t" \
        "s_barrier\n\t" \
        "ds_read_b32 %[p1], %[ast]\n\t" \
        "ds_read_b32 %[p2], %[ast] offset:4\n\t" \
        "v_mfma_f32_16x16x32_bf16 %[c2], %[f0], a[%c[N]+32:%c[N]+35], 0\n\t" \
        "v_mfma_f32_16x16x32_bf16 %[c3], %[f0], a[%c[N]+48:%c[N]+51], 0\n\t" \
        "v_mfma_f32_16x16x32_bf16 %[c0], %[f1], a[%c[N]+4:%c[N]+7], %[c0]\n\t" \
        "v_mfma_f32_16x16x32_bf16 %[c1], %[f1], a[%c[N]+20:%c[N]+23], %[c1]\n\t" \
        "s_waitcnt lgkmcnt(0)\n\t" \
        "v_add_f32_dpp %[p1], %[p1], %[p1] quad_perm:[1,0,3,2] row_mask:0xf bank_mask:0xf\n\t" \
        "v_add_f32_dpp %[p2], %[p2], %[p2] quad_perm:[1,0,3,2] row_mask:0xf bank_mask:0xf\n\t" \
        "v_mfma_f32_16x16x32_bf16 %[c2], %[f1], a[%c[N]+36:%c[N]+39], %[c2]\n\t" \
        "v_add_f32_dpp %[p1], %[p1], %[p1] quad_perm:[2,3,0,1] row_mask:0xf bank_mask:0xf\n\t" \
        "v_add_f32_dpp %[p2], %[p2], %[p2] quad_perm:[2,3,0,1] row_mask:0xf bank_mask:0xf\n\t" \
        "v_mfma_f32_16x16x32_bf16 %[c3], %[f1], a[%c[N]+52:%c[N]+55], %[c3]\n\t" \
        "v_add_f32_dpp %[p1], %[p1], %[p1] row_half_mirror row_mask:0xf bank_mask:0xf\n\t" \
        "v_add_f32_dpp %[p2], %[p2], %[p2] row_half_mirror row_mask:0xf bank_mask:0xf\n\t" \
        "v_mfma_f32_16x16x32_bf16 %[c0], %[f2], a[%c[N]+8:%c[N]+11], %[c0]\n\t" \
        "v_add_f32_dpp %[p1], %[p1], %[p1] row_mirror row_mask:0xf bank_mask:0xf\n\t" \
        "v_add_f32_dpp %[p2], %[p2], %[p2] row_mirror row_mask:0xf bank_mask:0xf\n\t" \
        "v_mfma_f32_16x16x32_bf16 %[c1], %[f2], a[%c[N]+24:%c[N]+27], %[c1]\n\t" \
        "v_mul_f32 %[p1], 0x3b000000, %[p1]\n\t" \
        "v_mul_f32 %[p2], 0x3b000000, %[p2]\n\t" \
        "v_mfma_f32_16x16x32_bf16 %[c2], %[f2], a[%c[N]+40:%c[N]+43], %[c2]\n\t" \
        "v_fma_f32 %[p2], -%[p1], %[p1], %[p2]\n\t" \
        "v_max_f32 %[p2], 0, %[p2]\n\t" \
        "v_mfma_f32_16x16x32_bf16 %[c3], %[f2], a[%c[N]+56:%c[N]+59], %[c3]\n\t" \
        "v_add_f32 %[p2], 0x3727c5ac, %[p2]\n\t" \
        "v_rsq_f32 %[p2], %[p2]\n\t" \
        "v_mfma_f32_16x16x32_bf16 %[c0], %[f3], a[%c[N]+12:%c[N]+15], %[c0]\n\t" \
        "v_mfma_f32_16x16x32_bf16 %[c1], %[f3], a[%c[N]+28:%c[N]+31], %[c1]\n\t" \
        "v_mul_f32 %[p1], %[p1], %[p2]\n\t" \
        "v_sub_f32 %[p1], 0, %[p1]\n\t" \
        "v_mfma_f32_16x16x32_bf16 %[c2], %[f3], a[%c[N]+44:%c[N]+47], %[c2]\n\t" \
        "v_mfma_f32_16x16x32_bf16 %[c3], %[f3], a[%c[N]+60:%c[N]+63], %[c3]\n\t" \
        "s_nop 7\n\t" "" \
        : [c0] "=&v"(acc[0]), [c1] "=&v"(acc[1]), [c2] "=&v"(acc[2]), [c3] "=&v"(acc[3]), [p1] "=&v"(tq), [p2] "=&v"(rstd) \
        : [f0] "v"(f[0]), [f1] "v"(f[1]), [f2] "v"(f[2]), [f3] "v"(f[3]), [ast] "v"(a_st), [N] "i"(NB) : "memory")
#define QKBLOCK(R) asm volatile("v_mfma_f32_16x16x32_bf16 %[s0], %[k0], %[q0], 0\n\t" \
        "v_mfma_f32_16x16x32_bf16 %[s1], %[k4], %[q0], 0\n\t" \
        "v_mfma_f32_16x16x32_bf16 %[s0], %[k1], %[q1], %[s0]\n\t" \
        "v_mfma_f32_16x16x32_bf16 %[s1], %[k5], %[q1], %[s1]\n\t" \
        "v_mfma_f32_16x16x32_bf16 %[s0], %[k2], %[q2], %[s0]\n\t" \
        "v_mfma_f32_16x16x32_bf16 %[s1], %[k6], %[q2], %[s1]\n\t" \
        "v_mfma_f32_16x16x32_bf16 %[s0], %[k3], %[q3], %[s0]\n\t" \
        "v_mfma_f32_16x16x32_bf16 %[s1], %[k7], %[q3], %[s1]\n\t" \
        "s_nop 7\n\t" "" \
        : [s0] "=&v"(sc0), [s1] "=&v"(sc1) \
        : [k0] "v"(R.kf[0]), [k1] "v"(R.kf[1]), [k2] "v"(R.kf[2]), [k3] "v"(R.kf[3]), [k4] "v"(R.kf[4]), [k5] "v"(R.kf[5]), [k6] "v"(R.kf[6]), [k7] "v"(R.kf[7]), [q0] "v"(qf[0]), [q1] "v"(qf[1]), [q2] "v"(qf[2]), [q3] "v"(qf[3]))
#define PVBLOCK(R) asm volatile("v_mfma_f32_16x16x32_bf16 %[o0], %[v0], %[p0], 0\n\t" \
        "v_mfma_f32_16x16x32_bf16 %[o1], %[v1], %[p0], 0\n\t" \
        "v_mfma_f32_16x16x32_bf16 %[o2], %[v2], %[p1], 0\n\t" \
        "v_mfma_f32_16x16x32_bf16 %[o3], %[v3], %[p1], 0\n\t" \
        "v_mfma_f32_16x16x32_bf16 %[o4], %[v4], %[p2], 0\n\t" \
        "v_mfma_f32_16x16x32_bf16 %[o5], %[v5], %[p2], 0\n\t" \
        "v_mfma_f32_16x16x32_bf16 %[o6], %[v6], %[p3], 0\n\t" \
        "v_mfma_f32_16x16x32_bf16 %[o7], %[v7], %[p3], 0\n\t" \
        "s_nop 7\n\t" "" \
        : [o0] "=&v"(oa[0][0]), [o1] "=&v"(oa[0][1]), [o2] "=&v"(oa[1][0]), [o3] "=&v"(oa[1][1]), [o4] "=&v"(oa[2][0]), [o5] "=&v"(oa[2][1]), [o6] "=&v"(oa[3][0]), [o7] "=&v"(oa[3][1]) \
        : [v0] "v"(R.vf[0]), [v1] "v"(R.vf[1]), [v2] "v"(R.vf[2]), [v3] "v"(R.vf[3]), [v4] "v"(R.vf[4]), [v5] "v"(R.vf[5]), [v6] "v"(R.vf[6]), [v7] "v"(R.vf[7]), [p0] "v"(pf[0]), [p1] "v"(pf[1]), [p2] "v"(pf[2]), [p3] "v"(pf[3]))
#define OBLOCK(NB) asm volatile("v_mfma_f32_16x16x32_bf16 %[L0], %[a0], a[%c[N]+0:%c[N]+3], 0\n\t" \
        "v_max_f32_dpp %[m], %[la], %[la] quad_perm:[1,0,3,2] row_mask:0xf bank_mask:0xf\n\t" \
        "v_mfma_f32_16x16x32_bf16 %[H0], %[a0], a[%c[N]+4:%c[N]+7], 0\n\t" \
        "s_nop 0\n\t" \
        "v_max_f32_dpp %[m], %[m], %[m] quad_perm:[2,3,0,1] row_mask:0xf bank_mask:0xf\n\t" \
        "v_mfma_f32_16x16x32_bf16 %[L1], %[a0], a[%c[N]+16:%c[N]+19], 0\n\t" \
        "s_nop 0\n\t" \
        "v_max_f32_dpp %[m], %[m], %[m] row_half_mirror row_mask:0xf bank_mask:0xf\n\t" \
        "v_mfma_f32_16x16x32_bf16 %[H1], %[a0], a[%c[N]+20:%c[N]+23], 0\n\t" \
        "v_sub_f32 %[m], %[la], %[m]\n\t" \
        "v_exp_f32 %[e], %[m]\n\t" \
        "v_mfma_f32_16x16x32_bf16 %[L2], %[a0], a[%c[N]+32:%c[N]+35], 0\n\t" \
        "s_nop 0\n\t" \
        "v_add_f32_dpp %[m], %[e], %[e] quad_perm:[1,0,3,2] row_mask:0xf bank_mask:0xf\n\t" \
        "v_mfma_f32_16x16x32_bf16 %[H2], %[a0], a[%c[N]+36:%c[N]+39], 0\n\t" \
        "s_nop 0\n\t" \
        "v_add_f32_dpp %[m], %[m], %[m] quad_perm:[2,3,0,1] row_mask:0xf bank_mask:0xf\n\t" \
        "v_mfma_f32_16x16x32_bf16 %[L3], %[a0], a[%c[N]+48:%c[N]+51], 0\n\t" \
        "s_nop 0\n\t" \
        "v_add_f32_dpp %[m], %[m], %[m] row_half_mirror row_mask:0xf bank_mask:0xf\n\t" \
        "v_mfma_f32_16x16x32_bf16 %[H3], %[a0], a[%c[N]+52:%c[N]+55], 0\n\t" \
        "v_rcp_f32 %[m], %[m]\n\t" \
        "v_mfma_f32_16x16x32_bf16 %[L0], %[a1], a[%c[N]+8:%c[N]+11], %[L0]\n\t" \
        "v_mul_f32 %[e], %[e], %[m]\n\t" \
        "ds_write_b32 %[aw], %[e]\n\t" \
        "v_mfma_f32_16x16x32_bf16 %[H0], %[a1], a[%c[N]+12:%c[N]+15], %[H0]\n\t" \
        "ds_read_b128 %[wt], %[ar]\n\t" \
        "v_mfma_f32_16x16x32_bf16 %[L1], %[a1], a[%c[N]+24:%c[N]+27], %[L1]\n\t" \
        "v_mfma_f32_16x16x32_bf16 %[H1], %[a1], a[%c[N]+28:%c[N]+31], %[H1]\n\t" \
        "v_mfma_f32_16x16x32_bf16 %[L2], %[a1], a[%c[N]+40:%c[N]+43], %[L2]\n\t" \
        "v_mfma_f32_16x16x32_bf16 %[H2], %[a1], a[%c[N]+44:%c[N]+47], %[H2]\n\t" \
        "v_mfma_f32_16x16x32_bf16 %[L3], %[a1], a[%c[N]+56:%c[N]+59], %[L3]\n\t" \
        "v_mfma_f32_16x16x32_bf16 %[H3], %[a1], a[%c[N]+60:%c[N]+63], %[H3]\n\t" \
        "s_waitcnt lgkmcnt(0)\n\t" \
        "s_nop 6\n\t" "" \
        : [L0] "=&v"(aL[0]), [H0] "=&v"(aH[0]), [L1] "=&v"(aL[1]), [H1] "=&v"(aH[1]), [L2] "=&v"(aL[2]), [H2] "=&v"(aH[2]), [L3] "=&v"(aL[3]), [H3] "=&v"(aH[3]), \
          [wt] "=&v"(wt), [m] "=&v"(tm), [e] "=&v"(te) \
        : [a0] "v"(oa0), [a1] "v"(oa1), [la] "v"(la), [aw] "v"(a_wtw), [ar] "v"(a_wtr), [N] "i"(NB) : "memory")
__device__ __forceinline__ float max3f(float a, float b, float c) { float r; asm("v_max3_f32 %0, %1, %2, %3" : "=v"(r) : "v"(a), "v"(b), "v"(c)); return r; }
__device__ __forceinline__ float max2f(float a, float b) { float r; asm("v_max_f32 %0, %1, %2" : "=v"(r) : "v"(a), "v"(b)); return r; }
__device__ __forceinline__ float x16_maxf(float x) {
    auto s = __builtin_amdgcn_permlane16_swap(__float_as_uint(x), __float_as_uint(x), false, false);
    x = max2f(__uint_as_float(s[0]), __uint_as_float(s[1]));
    auto t = __builtin_amdgcn_permlane32_swap(__float_as_uint(x), __float_as_uint(x), false, false);
    return max2f(__uint_as_float(t[0]), __uint_as_float(t[1]));
}
constexpr unsigned OOB = 0x80000000u;
template <int CTRL> __device__ __forceinline__ void dpp_acc(float& x) { x += dpp<CTRL>(x); asm("" : "+v"(x)); }
#define UNLIKELY(x) __builtin_expect(!!(x), 0)
#define BAR() do { asm volatile("s_waitcnt lgkmcnt(0)" ::: "memory"); __builtin_amdgcn_s_barrier(); asm volatile("" ::: "memory"); } while (0)
#define VMWAIT(n) asm volatile("s_waitcnt vmcnt(" #n ")" ::: "memory")
template <int N> __device__ __forceinline__ void delay() {
#pragma unroll
    for (int i = 0; i < N; ++i) asm volatile("s_nop 15");
}

__device__ __forceinline__ void dma16(const unsigned char* sbase, unsigned voff, unsigned lds) {
    unsigned keep;
    asm volatile("s_mov_b32 %0, m0\n\ts_mov_b32 m0, %3\n\ts_nop 2\n\tglobal_load_lds_dwordx4 %1, %2" W_POL "\n\ts_mov_b32 m0, %0"
                 : "=&s"(keep) : "v"(voff), "s"(sbase), "s"(lds) : "memory");
}
__device__ __forceinline__ void dma16s(const unsigned char* sbase, unsigned voff, unsigned lds) {
    unsigned keep;
    asm volatile("s_mov_b32 %0, m0\n\ts_mov_b32 m0, %3\n\ts_nop 2\n\tglobal_load_lds_dwordx4 %1, %2 sc1\n\ts_mov_b32 m0, %0"
                 : "=&s"(keep) : "v"(voff), "s"(sbase), "s"(lds) : "memory");
}
__device__ __forceinline__ void dma16x4(const unsigned char* sbase, unsigned v0, unsigned v1, unsigned v2, unsigned v3, unsigned lds) {
    unsigned keep;
    asm volatile("s_mov_b32 %0, m0\n\ts_mov_b32 m0, %6\n\ts_nop 2\n\t"
                 "global_load_lds_dwordx4 %1, %5" KV_POL "\n\tglobal_load_lds_dwordx4 %2, %5 offset:1024" KV_POL "\n\t"
                 "global_load_lds_dwordx4 %3, %5 offset:2048" KV_POL "\n\tglobal_load_lds_dwordx4 %4, %5 offset:3072" KV_POL "\n\ts_mov_b32 m0, %0"
                 : "=&s"(keep) : "v"(v0), "v"(v1), "v"(v2), "v"(v3), "s"(sbase), "s"(lds) : "memory");
}
template <class T> __device__ __forceinline__ T opq_s(T v) { asm volatile("" : "+s"(v)); return v; }
template <class T> __device__ __forceinline__ T opq_v(T v) { asm volatile("" : "+v"(v)); return v; }
#define BLD4(dst, voff, soff, imm) asm volatile("buffer_load_dwordx4 %0, %1, %2, %3 offen offset:" #imm " sc1" : "=&v"(dst) : "v"(voff), "s"(rsv), "s"(soff) : "memory")
#define BLD2(dst, voff, soff, imm) asm volatile("buffer_load_dwordx2 %0, %1, %2, %3 offen offset:" #imm " sc1" : "=&v"(dst) : "v"(voff), "s"(rsv), "s"(soff) : "memory")
#define BLD1(dst, voff, soff, imm) asm volatile("buffer_load_dword %0, %1, %2, %3 offen offset:" #imm " sc1" : "=&v"(dst) : "v"(voff), "s"(rsv), "s"(soff) : "memory")
#define BLD4W(dst, voff, soff) asm volatile("buffer_load_dwordx4 %0, %1, %2, %3 offen sc1\n\ts_waitcnt vmcnt(0)" : "=&v"(dst) : "v"(voff), "s"(rsv), "s"(soff) : "memory")
#define BLD2W(dst, voff, soff) asm volatile("buffer_load_dwordx2 %0, %1, %2, %3 offen sc1\n\ts_waitcnt vmcnt(0)" : "=&v"(dst) : "v"(voff), "s"(rsv), "s"(soff) : "memory")
#define BLDOW(a0, a1, la, v0, v1, soff, soffl) asm volatile("buffer_load_dwordx4 %0, %3, %5, %6 offen sc1\n\tbuffer_load_dwordx4 %1, %3, %5, %6 offen offset:128 sc1\n\tbuffer_load_dword %2, %4, %5, %7 offen sc1\n\ts_waitcnt vmcnt(0)" \
    : "=&v"(a0), "=&v"(a1), "=&v"(la) : "v"(v0), "v"(v1), "s"(rsv), "s"(soff), "s"(soffl) : "memory")
#define BLDXW(a0, a1, v0, v1, soff) asm volatile("buffer_load_dwordx4 %0, %2, %4, %5 offen sc1\n\tbuffer_load_dwordx4 %1, %3, %4, %5 offen sc1\n\ts_waitcnt vmcnt(0)" \
    : "=&v"(a0), "=&v"(a1) : "v"(v0), "v"(v1), "s"(rsv), "s"(soff) : "memory")
__device__ __forceinline__ v4u tr2(LAS unsigned char* p0, LAS unsigned char* p1) {
    typedef short v4i16_t __attribute__((ext_vector_type(4)));
    v4i16_t a = __builtin_amdgcn_ds_read_tr16_b64_v4i16((LAS v4i16_t*)p0);
    v4i16_t b = __builtin_amdgcn_ds_read_tr16_b64_v4i16((LAS v4i16_t*)p1);
    v2u x = __builtin_bit_cast(v2u, a), y = __builtin_bit_cast(v2u, b);
    return v4u{x.x, x.y, y.x, y.y};
}


struct Res { v4u kf[8], vf[8]; float cs; };
template <int V> using IC = std::integral_constant<int, V>;

__global__ void __launch_bounds__(256, 1) mega(Params p) {
    extern __shared__ __attribute__((aligned(16))) unsigned char smem[];
    LAS unsigned char* L = (LAS unsigned char*)smem;
    const int c = blockIdx.x, tid = threadIdx.x, lane = tid & 63, w = __builtin_amdgcn_readfirstlane(tid >> 6);
    const int kvh = c & 7, s = (c >> 3) & 7, b = c >> 6, grp = b * 8 + kvh;
    const int g = lane >> 4, r16 = lane & 15, half = lane >> 5, i32 = lane & 31;
    const __amdgpu_buffer_rsrc_t rs = __builtin_amdgcn_make_buffer_rsrc(p.ws, 0, WS_END, 0x00020000);
#define ST4(v, off) __builtin_amdgcn_raw_buffer_store_b128((v), rs, (off), 0, AUX_SC1)
#define ST2(v, off) __builtin_amdgcn_raw_buffer_store_b64((v), rs, (off), 0, AUX_SC1)
#define ST1(v, off) __builtin_amdgcn_raw_buffer_store_b32((v), rs, (off), 0, AUX_SC1)
#define ST2S(v, voff, soff) __builtin_amdgcn_raw_buffer_store_b64((v), rs, (voff), (soff), AUX_SC1)
#define ST1S(v, voff, soff) __builtin_amdgcn_raw_buffer_store_b32((v), rs, (voff), (soff), AUX_SC1)
    KEEP_ACC();
    const unsigned long long wsa = (unsigned long long)p.ws;
    const v4u rsv = {(unsigned)wsa, (unsigned)(wsa >> 32) & 0xFFFFu, (unsigned)WS_END, 0x00020000u};

    auto stage_w = [&](const u16* wmat, const unsigned ld, const unsigned coloff, const int u, const unsigned ldsb, const int h) {
        const unsigned ln = opq_v((unsigned)lane);
        const unsigned wrow = ln >> 3, wpos = ln & 7, wf = (wrow >> 1) & 1;
        const unsigned v0 = wrow * ld + ((((wpos >> 1) ^ wf) << 1) | (wpos & 1)) * 16;
        const unsigned v1 = wrow * ld + ((((wpos >> 1) ^ (wf | 2)) << 1) | (wpos & 1)) * 16;
        const unsigned char* wb = (const unsigned char*)(opq_s(wmat) + coloff) + (size_t)(u * 128 + h * 64) * ld;
        const int bb = opq_s(b);
#pragma unroll 1
        for (int i = 0; i < 8; i += 2) {
            const int pc = (i + 2 * bb) & 7;
            dma16(wb + (size_t)(pc * 8) * ld, v0, ldsb + h * 8192 + pc * 1024);
            dma16(wb + (size_t)(pc * 8 + 8) * ld, v1, ldsb + h * 8192 + pc * 1024 + 1024);
        }
    };
    const unsigned kvrow = (unsigned)((b * HKV + kvh) * SK + (SK - W) + s * 128);
    auto stage_v = [&](const u16* cache, const int u, const unsigned ldsb) {
        const unsigned ln = opq_v((unsigned)lane), gg = ln >> 4, rr = ln & 15;
        const unsigned char* sb = (const unsigned char*)opq_s(cache) + (size_t)(kvrow + u * 32) * 256;
        const unsigned v0 = gg * 256 + ((((rr >> 1) ^ gg) << 1) | (rr & 1)) * 16, v1 = gg * 256 + ((((rr >> 1) ^ (4 + gg)) << 1) | (rr & 1)) * 16;
        dma16x4(sb, v0, v1, v0, v1, ldsb);
        dma16x4(sb + 4096, v0, v1, v0, v1, ldsb + 4096);
    };
    auto consume_w = [&](auto NC, const unsigned ldsb) {
        constexpr int N = decltype(NC)::value;
        const unsigned ln = opq_v((unsigned)lane), gg = ln >> 4, qq = (ln & 15) >> 2, pp = ln & 3;
        const unsigned f = (qq >> 1) | ((gg & 1) << 1);
        const unsigned base = ldsb + (8 * gg + qq) * 128 + (pp >> 1) * 16 + (pp & 1) * 8;
        tr_col<N>(base + (0 ^ f) * 32); tr_col<N + 16>(base + (1 ^ f) * 32); tr_col<N + 32>(base + (2 ^ f) * 32); tr_col<N + 48>(base + (3 ^ f) * 32);
        asm volatile("s_waitcnt lgkmcnt(0)" ::: "memory");
    };
#define LOAD_K(R, cache) do { \
        const unsigned ln_ = opq_v((unsigned)lane); \
        const unsigned char* kb_ = (const unsigned char*)opq_s(cache) + (size_t)(kvrow + opq_s(w) * 32) * 256; \
        const unsigned v_ = (ln_ & 15) * 256 + (ln_ >> 4) * 16, v2_ = v_ + 4096; \
        asm volatile("s_nop 4\n\tglobal_load_dwordx4 %0, %8, %10" KV_POL "\n\tglobal_load_dwordx4 %1, %8, %10 offset:64" KV_POL "\n\tglobal_load_dwordx4 %2, %8, %10 offset:128" KV_POL "\n\tglobal_load_dwordx4 %3, %8, %10 offset:192" KV_POL "\n\t" \
                     "global_load_dwordx4 %4, %9, %10" KV_POL "\n\tglobal_load_dwordx4 %5, %9, %10 offset:64" KV_POL "\n\tglobal_load_dwordx4 %6, %9, %10 offset:128" KV_POL "\n\tglobal_load_dwordx4 %7, %9, %10 offset:192" KV_POL \
                     : "=&v"(R.kf[0]), "=&v"(R.kf[1]), "=&v"(R.kf[2]), "=&v"(R.kf[3]), "=&v"(R.kf[4]), "=&v"(R.kf[5]), "=&v"(R.kf[6]), "=&v"(R.kf[7]) \
                     : "v"(v_), "v"(v2_), "s"(kb_) : "memory"); } while (0)
#define PIN_K(R) asm volatile("; COVERED_K" : "+v"(R.kf[0]), "+v"(R.kf[1]), "+v"(R.kf[2]), "+v"(R.kf[3]), "+v"(R.kf[4]), "+v"(R.kf[5]), "+v"(R.kf[6]), "+v"(R.kf[7]))
    auto consume_v = [&](v4u* dst, const unsigned off) {
        const unsigned ln = opq_v((unsigned)lane), gg = ln >> 4, qq = (ln & 15) >> 2, pp = ln & 3;
        const unsigned vrow = 4 * gg + qq;
#pragma unroll
        for (int i = 0; i < 2; ++i) {
            LAS unsigned char* a = L + off + vrow * 256 + (((unsigned)(2 * w + i) ^ (vrow & 7)) * 32) + (pp >> 1) * 16 + (pp & 1) * 8;
#pragma unroll
            for (int t = 0; t < 4; ++t) dst[t * 2 + i] = tr2(a + t * RW, a + t * RW + 4096);
        }
    };
    const unsigned wq_col = (unsigned)(kvh * 512 + s * 64), wo_col = (unsigned)(kvh * 512 * 512 + s * 64);
    const unsigned myr = (unsigned)(w * RW);

    Res RA, RB;

    const unsigned xcol = (unsigned)(128 * w + 8 * r16 + 4 * (g >> 1) + 2 * (g & 1));
    unsigned xpk = 0;
    v2u gen = {0u, 0u};
    if (w == 0) {
        dma16s(p.ws + WS_GEN, opq_v((unsigned)lane) * 16, L_GENB);
        dma16((const unsigned char*)p.x + b * 1024, opq_v((unsigned)lane) * 16, L_XS);
    }
    stage_w(p.wq0, 8192, wq_col, w, myr + U0, 0); stage_w(p.wq0, 8192, wq_col, w, myr + U0, 1);
    LOAD_K(RA, p.kc0);
    stage_v(p.vc0, w, myr + U1 + 8192);
    ((LAS unsigned*)(L + L_ON))[lane] = 0x3F803F80u;

    const unsigned xg_v0 = (unsigned)(g * XB_K + w * 256 + r16 * 16), xg_v1 = xg_v0 + 4 * XB_K;
    const unsigned vq_st = ((lane & 1) == 0 && g == w) ? (unsigned)(lane * 2) : OOB;
    const unsigned vo_st = r16 < 4 ? (unsigned)(r16 * 256 + g * 8) : OOB;
    const unsigned vl_st = (lane < 4) ? (unsigned)(lane * 32) : OOB;
    const unsigned vx_st = (unsigned)((2 * w + half) * XB_R + i32 * 4);

    auto x_sum = [&](const v4u& a0, const v4u& a1, const bool active, float& o0, float& o1) -> bool {
        float v[8];
#pragma unroll
        for (int j = 0; j < 4; ++j) { v[2 * j] = bflo(a1[j]) + bflo(a0[j]); v[2 * j + 1] = bfhi(a1[j]) + bfhi(a0[j]); }
        float u[4];
#pragma unroll
        for (int j = 0; j < 4; ++j) u[j] = swap32_add(v[j], v[j + 4]);
        o0 = swap16_add(u[0], u[2]); o1 = swap16_add(u[1], u[3]);
        const bool ok = !active || !__builtin_isunordered(o0, o1);
        if (__all(ok)) return true;
        const unsigned m = max(mx4(a0), mx4(a1));
        return __all(!active || m != EMPTY);
    };
    auto hello_ok = [&](const v2u& h) -> bool { return __all(h.x == gen.x && h.y == gen.y); };
    bool hello64 = false;

#pragma unroll 1
    for (int lp = 0; lp < NLAY; ++lp) {
        const int l = opq_s(lp);
        const bool TA = (lp & 1) == 0;
        const bool f0 = lp == 0, f1 = lp == 1;
        const int so_q = WS_QB + l * QB_L + grp * 1024, so_o = WS_OB + l * OB_L + grp * OB_G, so_l = WS_LB + l * LB_L + grp * 128, so_x = WS_XB + l * XB_L + b * XB_B;
        if (f0) {
            VMWAIT(16);
            BAR();
            {
                const v2u g0 = *(LAS v2u*)(L + L_GENB + b * 256);
                const unsigned long long g1 = (((unsigned long long)g0.y << 32) | g0.x) + 1ull;
                gen = v2u{(unsigned)g1, (unsigned)(g1 >> 32)};
            }
            xpk = *(LAS unsigned*)(L + L_XS + xcol * 2);
        }
        KEEP_ACC();
        if (lp > 0) *(LAS unsigned*)(L + L_XS + xcol * 2) = xpk;
        float rstd, tq;
        f32x4 acc[4];
        {
            LAS unsigned char* xb = L + (((f0 || f1) && r16 == 1) ? L_ON : L_XS + w * 256) + g * 16;
            v4u f[4];
#pragma unroll
            for (int t = 0; t < 4; ++t) f[t] = *(LAS v4u*)(xb + t * 64);
            SB();
            {
                const float x0 = bflo(xpk), x1 = bfhi(xpk);
                float p1 = x0 + x1, p2 = x0 * x0 + x1 * x1;
                dpp_acc<XOR1>(p1); dpp_acc<XOR1>(p2); dpp_acc<XOR2>(p1); dpp_acc<XOR2>(p2);
                dpp_acc<HMIR>(p1); dpp_acc<HMIR>(p2); dpp_acc<MIR>(p1); dpp_acc<MIR>(p2);
                *(LAS f32x2*)(L + L_ST + (w * 4 + g) * 8) = f32x2{p1, p2};
            }
            SB();
            if (f0) consume_w(IC<0>{}, myr + U0);
            const unsigned a_st = (unsigned)(L_ST + r16 * 8);
            if (TA) QBLOCK(0); else QBLOCK(128);
            LAS float* rq = (LAS float*)(L + L_RQ) + w * 64 + r16;
            rq[0] = acc[0][0]; rq[16] = acc[1][0]; rq[32] = acc[2][0]; rq[48] = acc[3][0];
            if (f0 || f1) {
                LAS float* rc = (LAS float*)(L + (g == 0 ? L_RC + w * 256 : L_TRASH + g * 256)) + r16;
                rc[0] = acc[0][1]; rc[16] = acc[1][1]; rc[32] = acc[2][1]; rc[48] = acc[3][1];
            }
        }
        BAR();
        KEEP_ACC();
        v4u qa0 = {0u, 0u, 0u, 0u};
        {
            LAS float* rq = (LAS float*)(L + L_RQ) + lane;
            const float raw = (rq[0] + rq[64]) + (rq[128] + rq[192]);
            if (f0 || f1) {
                LAS float* rc = (LAS float*)(L + L_RC) + lane;
                const float cs = (rc[0] + rc[64]) + (rc[128] + rc[192]);
                if (f0) RA.cs = cs; else RB.cs = cs;
            }
            const float q = raw * rstd + tq * ((lp & 1) ? RB.cs : RA.cs);
            const unsigned qo = noempty(cvtpk(q, dpp<XOR1>(q)));
            {
                const int soff = opq_s(so_q + w * 256);
                const unsigned voff = (unsigned)(r16 * 16);
                if (f0) {
                    ST2S((v2u{qo, gen.x}), vq_st == OOB ? OOB : (unsigned)((lane >> 1) * 8), WS_QT + grp * 2048 + s * 256);
                    stage_w(p.wo0, 1024, wo_col, w, myr + U0, 0); stage_w(p.wo0, 1024, wo_col, w, myr + U0, 1);
                    {
                        const v4u e = {EMPTY, EMPTY, EMPTY, EMPTY};
                        const unsigned ln = opq_v((unsigned)lane);
#pragma unroll
                        for (int l4 = 0; l4 < NLAY; l4 += 4) {
                            ST4(e, WS_OB + (l4 + w) * OB_L + grp * OB_G + s * OB_P + ln * 16);
                            ST4(e, WS_XB + (l4 + w) * XB_L + b * XB_B + (ln >> 3) * XB_R + kvh * XB_K + s * 128 + (ln & 7) * 16);
                        }
                        if (w == 0) ST4(e, (ln >> 3) < NLAY ? (unsigned)(WS_QB + (ln >> 3) * QB_L + grp * 1024 + s * 128 + (ln & 7) * 16) : OOB);
                        if (w == 1) ST1(EMPTY, (ln >> 2) < NLAY ? (unsigned)(WS_LB + (ln >> 2) * LB_L + grp * 128 + (ln & 3) * 32 + s * 4) : OOB);
                    }
                    const int sofft = opq_s(WS_QT + grp * 2048 + w * 512);
                    const unsigned vofft = (unsigned)(i32 * 16);
                    v4u t;
                    BLD4(t, vofft, sofft, 0);
                    LOAD_K(RB, p.kc1);
                    stage_v(p.vc1, w, myr + U1);
                    asm volatile("s_waitcnt vmcnt(16) ; COVER" : "+v"(t) :: "memory");
                    PIN_K(RA);
                    if (UNLIKELY(!__all(t.y == gen.x && t.w == gen.x))) { for (unsigned sp = 0; sp < SPIN_CAP; ++sp) { BLD4W(t, vofft, sofft); if (__all(t.y == gen.x && t.w == gen.x)) break; } }
                    *(LAS v2u*)(L + L_QS + w * 256 + i32 * 8) = v2u{t.x, t.z};
                } else {
                    ST1S(qo, vq_st, so_q + s * 128);
                    delay<DLY_Q>();
                    BLD4W(qa0, voff, soff);
                }
                if (!f0 && UNLIKELY(!__all(mx4(qa0) != EMPTY))) {
                    for (unsigned sp = 0; sp < SPIN_CAP; ++sp) { BLD4W(qa0, voff, soff); if (__all(mx4(qa0) != EMPTY)) break; }
                }
            }
        }
        KEEP_ACC();
        if (!f0) *(LAS v4u*)(L + L_QS + w * 256 + r16 * 16) = qa0;
        BAR();
        if (f0 && tid == 0) ST2(gen, WS_HELLO + (b * 64 + kvh * 8 + s) * 8);
        f32x4 sc0, sc1;
        {
            LAS unsigned char* qb = L + L_QS + (lane & 3) * 256 + g * 16;
            v4u qf[4];
#pragma unroll
            for (int t = 0; t < 4; ++t) qf[t] = *(LAS v4u*)(qb + t * 64);
            SB();
            if (TA) QKBLOCK(RA); else QKBLOCK(RB);
            SB();
            if (f0) consume_v(RA.vf, U1 + 8192);
        }
        KEEP_ACC();
        constexpr float CS = 0.08838834764831845f * 1.4426950408889634f;
        float mw = max3f(max3f(sc0[0], sc0[1], sc0[2]), max3f(sc0[3], sc1[0], sc1[1]), max2f(sc1[2], sc1[3]));
        mw = x16_maxf(mw) * CS;
        float pe[8];
#pragma unroll
        for (int j = 0; j < 4; ++j) { pe[j] = __builtin_amdgcn_exp2f(sc0[j] * CS - mw); pe[4 + j] = __builtin_amdgcn_exp2f(sc1[j] * CS - mw); }
        {
            const v4u pf = {cvtpk(pe[0], pe[1]), cvtpk(pe[2], pe[3]), cvtpk(pe[4], pe[5]), cvtpk(pe[6], pe[7])};
            *(LAS v4u*)(L + L_PS + w * 256 + (lane & 3) * 64 + g * 16) = pf;
            SB();
            float lw = ((pe[0] + pe[1]) + (pe[2] + pe[3])) + ((pe[4] + pe[5]) + (pe[6] + pe[7]));
            lw = x16_sum(lw);
            *(LAS f32x2*)(L + L_ML + (lane & 3) * 32 + w * 8) = f32x2{mw, lw};
        }
        KEEP_ACC();
        BAR();
        v4u oa0 = {0u, 0u, 0u, 0u}, oa1 = oa0; unsigned la = 0;
        {
            LAS unsigned char* pb = L + L_PS + (lane & 3) * 64 + g * 16;
            const f32x4 ml0 = *(LAS f32x4*)(L + L_ML + (lane & 3) * 32), ml1 = *(LAS f32x4*)(L + L_ML + (lane & 3) * 32 + 16);
            v4u pf[4];
#pragma unroll
            for (int t = 0; t < 4; ++t) pf[t] = *(LAS v4u*)(pb + t * 256);
            SB();
            const float M = max2f(max3f(ml0[0], ml0[2], ml1[0]), ml1[2]);
            const float a0 = __builtin_amdgcn_exp2f(ml0[0] - M), a1 = __builtin_amdgcn_exp2f(ml0[2] - M);
            const float a2 = __builtin_amdgcn_exp2f(ml1[0] - M), a3 = __builtin_amdgcn_exp2f(ml1[2] - M);
            const float Ls = (a0 * ml0[1] + a1 * ml0[3]) + (a2 * ml1[1] + a3 * ml1[3]);
            const float inv = __builtin_amdgcn_rcpf(Ls);
            float lam = M + __builtin_amdgcn_logf(Ls);
            float b0 = a0 * inv, b1 = a1 * inv, b2 = a2 * inv, b3 = a3 * inv;
            PIN(b0); PIN(b1); PIN(b2); PIN(b3); PIN(lam);
            SB();
            f32x4 oa[4][2];
            if (TA) PVBLOCK(RA); else PVBLOCK(RB);
            f32x4 o0 = oa[0][0] * b0, o1 = oa[0][1] * b0;
            o0 += oa[1][0] * b1; o1 += oa[1][1] * b1;
            o0 += oa[2][0] * b2; o1 += oa[2][1] * b2;
            o0 += oa[3][0] * b3; o1 += oa[3][1] * b3;
            const v2u w0 = {noempty(cvtpk(o0[0], o0[1])), noempty(cvtpk(o0[2], o0[3]))};
            const v2u w1 = {noempty(cvtpk(o1[0], o1[1])), noempty(cvtpk(o1[2], o1[3]))};
            {
                const int ob = so_o + s * OB_P;
                ST2S(w0, vo_st, ob + w * 64);
                ST2S(w1, vo_st, ob + w * 64 + 32);
                if (w == 0) ST1S(noempty(__float_as_uint(lam)), vl_st, so_l + s * 4);
                const int soff = opq_s(so_o + w * 256), soffl = opq_s(so_l + w * 32);
                const unsigned voff = (unsigned)((r16 & 7) * OB_P + (r16 >> 3) * 64 + g * 16);
                const unsigned voffl = (unsigned)((lane & 7) * 4);
                if (f0) {
                    v2u h;
                    const int hoff = opq_s(WS_HELLO + (b * 64) * 8);
                    const unsigned hv = (unsigned)(lane * 8);
                    const bool mine = (lane >> 3) == kvh;
                    BLD2(h, hv, hoff, 0);
                    consume_w(IC<64>{}, myr + U0);
                    stage_w(p.wq1, 8192, wq_col, w, myr + U0, 0); stage_w(p.wq1, 8192, wq_col, w, myr + U0, 1);
                    asm volatile("s_waitcnt vmcnt(16) ; COVER" : "+v"(h) :: "memory");
                    PIN_K(RB);
                    BAR();
                    consume_v(RB.vf, U1);
                    BAR();
                    if (UNLIKELY(!__all(!mine || (h.x == gen.x && h.y == gen.y)))) { for (unsigned sp = 0; sp < SPIN_CAP; ++sp) { BLD2W(h, hv, hoff); if (__all(!mine || (h.x == gen.x && h.y == gen.y))) break; } }
                    hello64 = hello_ok(h);
                    BLD4(oa0, voff, soff, 0); BLD4(oa1, voff, soff, 128); BLD1(la, voffl, soffl, 0);
                    stage_w(p.wo1, 1024, wo_col, w, myr + U1, 0); stage_w(p.wo1, 1024, wo_col, w, myr + U1, 1);
                    asm volatile("s_waitcnt vmcnt(16) ; COVER" : "+v"(oa0), "+v"(oa1), "+v"(la) :: "memory");
                } else if (f1) {
                    delay<DLY_O - 2>();
                    consume_w(IC<192>{}, myr + U1);
                    BLDOW(oa0, oa1, la, voff, voffl, soff, soffl);
                } else {
                    delay<DLY_O>();
                    BLDOW(oa0, oa1, la, voff, voffl, soff, soffl);
                }
                if (UNLIKELY(!__all(max(max(mx4(oa0), mx4(oa1)), la) != EMPTY))) {
                    for (unsigned sp = 0; sp < SPIN_CAP; ++sp) { BLDOW(oa0, oa1, la, voff, voffl, soff, soffl); if (__all(max(max(mx4(oa0), mx4(oa1)), la) != EMPTY)) break; }
                }
            }
        }
        KEEP_ACC();
        {
            f32x4 aL[4], aH[4];
            f32x4 wt; float tm, te;
            const unsigned a_wtw = (unsigned)(L_WT + w * 32 + (lane & 7) * 4), a_wtr = (unsigned)(L_WT + w * 32 + (g & 1) * 16);
            if (TA) OBLOCK(64); else OBLOCK(192);
            const f32x2 z2 = {0.f, 0.f};
            const f32x2 wL0 = half ? z2 : f32x2{wt[0], wt[1]}, wL1 = half ? z2 : f32x2{wt[2], wt[3]};
            const f32x2 wH0 = half ? f32x2{wt[0], wt[1]} : z2, wH1 = half ? f32x2{wt[2], wt[3]} : z2;
            float v[4];
#pragma unroll
            for (int n = 0; n < 4; ++n) {
                f32x2 t = f32x2{aL[n][0], aL[n][1]} * wL0;
                t += f32x2{aL[n][2], aL[n][3]} * wL1; t += f32x2{aH[n][0], aH[n][1]} * wH0; t += f32x2{aH[n][2], aH[n][3]} * wH1;
                v[n] = t.x + t.y;
            }
            const float u0 = swap32_add(v[0], v[1]), u1 = swap32_add(v[2], v[3]);
            const float tot = swap16_add(u0, u1);
            *((LAS float*)(L + L_RX) + w * 64 + ((g & 1) * 2 + (g >> 1)) * 16 + r16) = tot;
        }
        BAR();
        KEEP_ACC();
        {
            LAS unsigned char* rx = L + L_RX + i32 * 8;
            const f32x2 sum = (*(LAS f32x2*)(rx) + *(LAS f32x2*)(rx + 256)) + (*(LAS f32x2*)(rx + 512) + *(LAS f32x2*)(rx + 768));
            const unsigned ov = noempty(cvtpk(sum.x, sum.y));
            {
                ST1S(ov, vx_st, so_x + kvh * XB_K + s * 128);
                const bool fin = lp == NLAY - 1;
                const bool act = !fin || (s == 0 && w == (kvh >> 1) && (r16 >> 3) == (kvh & 1));
                if (!fin || (s == 0 && w == (kvh >> 1))) {
                    const int soff = opq_s(so_x + kvh * XB_R);
                    const unsigned v0 = act ? xg_v0 : OOB, v1 = act ? xg_v1 : OOB;
                    v4u a0, a1;
                    if (f0) {
                        if (UNLIKELY(!hello64)) {
                            v2u h;
                            const int hoff = opq_s(WS_HELLO + (b * 64) * 8);
                            const unsigned hv = (unsigned)(lane * 8);
                            for (unsigned sp = 0; sp < SPIN_CAP; ++sp) { BLD2W(h, hv, hoff); if (hello_ok(h)) break; }
                        }
                        if ((c & 63) == 0 && tid == 0) ST2(gen, WS_GEN + b * 256);
                        consume_w(IC<128>{}, myr + U0);
                        delay<DLY_X - 4>();
                        BLDXW(a0, a1, v0, v1, soff);
                    } else {
                        delay<DLY_X>();
                        BLDXW(a0, a1, v0, v1, soff);
                    }
                    float o0, o1;
                    if (UNLIKELY(!x_sum(a0, a1, act, o0, o1))) {
                        for (unsigned sp = 0; sp < SPIN_CAP; ++sp) { BLDXW(a0, a1, v0, v1, soff); if (x_sum(a0, a1, act, o0, o1)) break; }
                    }
                    xpk = cvtpk(bflo(xpk) + o0, bfhi(xpk) + o1);
                    if (fin && act) *(GAS unsigned*)((GAS unsigned char*)p.out + (b * 512 + xcol) * 2) = xpk;
                }
            }
        }
    }
}
}

extern "C" void kernel_launch(void* const* d_in, const int* in_sizes, int n_in, void* d_out, int out_size, void* d_ws, size_t ws_size, hipStream_t stream) {
    mk::Params p;
    p.x = (const u16*)d_in[0]; p.kc0 = (const u16*)d_in[1]; p.vc0 = (const u16*)d_in[2]; p.wq0 = (const u16*)d_in[3]; p.wo0 = (const u16*)d_in[4];
    p.kc1 = (const u16*)d_in[6]; p.vc1 = (const u16*)d_in[7]; p.wq1 = (const u16*)d_in[8]; p.wo1 = (const u16*)d_in[9];
    p.out = (u16*)d_out; p.ws = (unsigned char*)d_ws;
    (void)hipFuncSetAttribute((const void*)mk::mega, hipFuncAttributeMaxDynamicSharedMemorySize, mk::LDS_BYTES);
    hipLaunchKernelGGL(mk::mega, dim3(256), dim3(256), mk::LDS_BYTES, stream, p);
}
```
